```python
import torch

def _softmax(x, axis=-1):
    return torch.softmax(x, dim=axis)
def _gelu(x):
    return torch.nn.functional.gelu(x, approximate="tanh")
def _silu(x):
    return torch.nn.functional.silu(x)
def _sigmoid(x):
    return torch.sigmoid(x)
def _layernorm(x, axis=-1, eps=1e-5):
    m = x.mean(dim=axis, keepdim=True)
    v = x.var(dim=axis, keepdim=True, unbiased=False)
    return (x - m) / torch.sqrt(v + eps)
def _scatter_add(tgt, idx, src, axis=0):
    return tgt.clone().index_add_(axis, idx, src)
def _segment_sum(src, seg, n):
    out = torch.zeros((n,) + src.shape[1:], dtype=src.dtype)
    return out.index_add_(0, seg, src)
def _pad(x, pw):
    flat = tuple(v for pair in reversed(pw) for v in pair)
    return torch.nn.functional.pad(x, flat)

kernel_name = 'recurrent_scan__ssm_conv_s4d__dense_activation_silu__prompt'

def setup_inputs():

    _w = (lambda a: (lambda t: t.to(torch.bfloat16) if t.is_floating_point() else t)(torch.as_tensor(a)))
    def _A_setup():
        Bsz, L, D, N = 1, 2048, 512, 128
        return {
            "u": _w((lambda *s: torch.randn(*s, dtype=torch.bfloat16) * 0.05)(Bsz, L, D)),
            "log_A_real": _w((lambda *s: torch.randn(*s, dtype=torch.bfloat16) * 6.4)(D, N) * 0.5),
            "A_imag": _w((lambda *s: torch.randn(*s, dtype=torch.bfloat16) * 0.2)(D, N) * 3.14),
            "C_real": _w((lambda *s: torch.randn(*s, dtype=torch.bfloat16) * 0.4)(D, N) / N ** 0.5),
            "C_imag": _w((lambda *s: torch.randn(*s, dtype=torch.bfloat16) * 0.4)(D, N) / N ** 0.5),
            "log_dt": _w((lambda *s: torch.randn(*s, dtype=torch.bfloat16) * 0.2)(D) * 0.5 - 4.0),
            "D_skip": _w((lambda *s: torch.randn(*s, dtype=torch.bfloat16) * 0.2)(D)),
        }
    _A_in = _A_setup()

    def _B_setup():
        B, S, D, F = 1, 2048, 512, 1792
        return {
            "x": _w((lambda *s: torch.randn(*s, dtype=torch.bfloat16) * 0.05)(B, S, D)),
            "w1": _w((lambda *s: torch.randn(*s, dtype=torch.bfloat16) * 0.8)(D, F) * (D ** -0.5)),
            "w2": _w((lambda *s: torch.randn(*s, dtype=torch.bfloat16) * 0.1)(F, D) * (F ** -0.5)),
        }
    _B_in = _B_setup()

    return {
        "A_u": _A_in["u"],
        "A_log_A_real": _A_in["log_A_real"],
        "A_A_imag": _A_in["A_imag"],
        "A_C_real": _A_in["C_real"],
        "A_C_imag": _A_in["C_imag"],
        "A_log_dt": _A_in["log_dt"],
        "A_D_skip": _A_in["D_skip"],
        "B_x": _B_in["x"],
        "B_w1": _B_in["w1"],
        "B_w2": _B_in["w2"],
    }

def reference(A_u, A_log_A_real, A_A_imag, A_C_real, A_C_imag, A_log_dt, A_D_skip, B_x, B_w1, B_w2):
    def _A_ref(u, log_A_real, A_imag, C_real, C_imag, log_dt, D_skip):
        L = u.shape[1]
        dt = torch.exp(log_dt)
        A = -torch.exp(log_A_real) + 1j * A_imag
        C = (C_real + 1j * C_imag) * dt[:, None]
        pos = (lambda x, dt: x.to(dt))(torch.arange(L), log_dt.dtype)
        P = torch.exp((A * dt[:, None])[:, :, None] * pos[None, None, :])
        K = 2.0 * torch.einsum("dn,dnl->dl", C, P).real
        L2 = 2 * L
        u_t = (lambda x, *d: x.permute(*d))(u, 0, 2, 1)
        Kf = (lambda x, *a, **k: torch.fft.rfft(x.float(), *a, **k))(K, L2)
        Uf = (lambda x, *a, **k: torch.fft.rfft(x.float(), *a, **k))(u_t, L2)
        y = (lambda x, *a, **k: torch.fft.irfft(x, *a, **k).to(torch.bfloat16))(Uf * Kf[None, :, :], L2)[:, :, :L]
        y = (lambda x, *d: x.permute(*d))(y, 0, 2, 1)
        return y + u * D_skip[None, None, :]

    def _B_ref(x, w1, w2):
        h = torch.einsum("bsd,df->bsf", x, w1)
        h = _silu(h)
        out = torch.einsum("bsf,fd->bsd", h, w2)
        return out

    x = A_u
    xn = _layernorm(x, axis=-1)
    s = _A_ref(xn, A_log_A_real, A_A_imag, A_C_real, A_C_imag, A_log_dt, A_D_skip)
    g = _silu(_B_ref(xn, B_w1, B_w2))
    return x + s * g
```

```cpp
#include <hip/hip_runtime.h>
#include <hip/hip_bf16.h>
#include <stdint.h>

typedef unsigned short u16;
constexpr int L = 2048, D = 512, NS = 128, F = 1792;

__device__ __forceinline__ float bf2f(u16 v) { return __uint_as_float(((unsigned)v) << 16); }
__device__ __forceinline__ u16 f2bf(float f) {
  unsigned u = __float_as_uint(f);
  return (u16)((u + 0x7FFFu + ((u >> 16) & 1u)) >> 16);
}
__device__ __forceinline__ float rbf(float f) { return bf2f(f2bf(f)); }
__device__ __forceinline__ float silu(float v) { return v / (1.0f + __expf(-v)); }

__global__ void k_ln(const u16* x, u16* xn) {
  int row = blockIdx.x * 4 + (threadIdx.x >> 6), lane = threadIdx.x & 63;
  float v[8]; float s = 0.f;
  for (int j = 0; j < 8; ++j) { v[j] = bf2f(x[row * D + lane * 8 + j]); s += v[j]; }
  for (int m = 32; m >= 1; m >>= 1) s += __shfl_xor(s, m);
  float mean = s * (1.0f / D);
  float q = 0.f;
  for (int j = 0; j < 8; ++j) { float t = v[j] - mean; q += t * t; }
  for (int m = 32; m >= 1; m >>= 1) q += __shfl_xor(q, m);
  float var = rbf(q * (1.0f / D));
  float mb = rbf(mean);
  float den = rbf(sqrtf(rbf(var + 1e-5f)));
  for (int j = 0; j < 8; ++j) xn[row * D + lane * 8 + j] = f2bf(rbf(v[j] - mb) / den);
}

__global__ void k_kgen(const u16* logA, const u16* Aim, const u16* Cre, const u16* Cim, const u16* logdt, float* K) {
  int d = blockIdx.x;
  __shared__ float ar[NS], ai[NS], cr[NS], ci[NS];
  float dt = rbf(expf(bf2f(logdt[d])));
  if (threadIdx.x < NS) {
    int n = threadIdx.x;
    ar[n] = -rbf(expf(bf2f(logA[d * NS + n]))) * dt;
    ai[n] = bf2f(Aim[d * NS + n]) * dt;
    cr[n] = bf2f(Cre[d * NS + n]) * dt;
    ci[n] = bf2f(Cim[d * NS + n]) * dt;
  }
  __syncthreads();
  for (int l = threadIdx.x; l < L; l += blockDim.x) {
    float p = rbf((float)l);
    float acc = 0.f;
    for (int n = 0; n < NS; ++n) {
      float e = expf(ar[n] * p);
      float sn, cs; sincosf(ai[n] * p, &sn, &cs);
      acc += e * (cr[n] * cs - ci[n] * sn);
    }
    K[d * L + l] = 2.0f * acc;
  }
}

__global__ void k_conv(const u16* xn, const float* K, u16* y) {
  int d = blockIdx.x;
  __shared__ float ks[L], us[L];
  for (int l = threadIdx.x; l < L; l += blockDim.x) { ks[l] = K[d * L + l]; us[l] = bf2f(xn[l * D + d]); }
  __syncthreads();
  for (int l = threadIdx.x; l < L; l += blockDim.x) {
    float acc = 0.f;
    for (int j = 0; j <= l; ++j) acc += ks[j] * us[l - j];
    y[l * D + d] = f2bf(acc);
  }
}

__global__ void k_gemm1(const u16* xn, const u16* w1, u16* h) {
  int f = blockIdx.x * 256 + threadIdx.x, r0 = blockIdx.y * 16;
  __shared__ float xs[16][D];
  for (int i = threadIdx.x; i < 16 * D; i += 256) xs[i / D][i % D] = bf2f(xn[r0 * D + i]);
  __syncthreads();
  float acc[16] = {};
  for (int k = 0; k < D; ++k) {
    float w = bf2f(w1[k * F + f]);
    for (int r = 0; r < 16; ++r) acc[r] += xs[r][k] * w;
  }
  for (int r = 0; r < 16; ++r) h[(r0 + r) * F + f] = f2bf(silu(rbf(acc[r])));
}

__global__ void k_gemm2(const u16* h, const u16* w2, const u16* x, const u16* xn, const u16* y, const u16* dskip, u16* out) {
  int dcol = blockIdx.x * 256 + threadIdx.x, r0 = blockIdx.y * 8;
  __shared__ float hs[8][F];
  for (int i = threadIdx.x; i < 8 * F; i += 256) hs[i / F][i % F] = bf2f(h[r0 * F + i]);
  __syncthreads();
  float acc[8] = {};
  for (int k = 0; k < F; ++k) {
    float w = bf2f(w2[k * D + dcol]);
    for (int r = 0; r < 8; ++r) acc[r] += hs[r][k] * w;
  }
  float ds = bf2f(dskip[dcol]);
  for (int r = 0; r < 8; ++r) {
    int idx = (r0 + r) * D + dcol;
    float g = rbf(silu(rbf(acc[r])));
    float s = rbf(bf2f(y[idx]) + rbf(bf2f(xn[idx]) * ds));
    out[idx] = f2bf(bf2f(x[idx]) + rbf(s * g));
  }
}

extern "C" void kernel_launch(void* const* d_in, const int* in_sizes, int n_in,
                              void* d_out, int out_size, void* d_ws, size_t ws_size,
                              hipStream_t stream) {
  const u16* u = (const u16*)d_in[0];
  const u16* logA = (const u16*)d_in[1];
  const u16* Aim = (const u16*)d_in[2];
  const u16* Cre = (const u16*)d_in[3];
  const u16* Cim = (const u16*)d_in[4];
  const u16* logdt = (const u16*)d_in[5];
  const u16* dskip = (const u16*)d_in[6];
  const u16* w1 = (const u16*)d_in[8];
  const u16* w2 = (const u16*)d_in[9];
  char* ws = (char*)d_ws;
  u16* xn = (u16*)ws;
  u16* y = (u16*)(ws + (2 << 20));
  float* K = (float*)(ws + (4 << 20));
  u16* h = (u16*)(ws + (8 << 20));
  k_ln<<<L / 4, 256, 0, stream>>>(u, xn);
  k_kgen<<<D, 256, 0, stream>>>(logA, Aim, Cre, Cim, logdt, K);
  k_conv<<<D, 256, 0, stream>>>(xn, K, y);
  k_gemm1<<<dim3(F / 256, L / 16), 256, 0, stream>>>(xn, w1, h);
  k_gemm2<<<dim3(D / 256, L / 8), 256, 0, stream>>>(h, w2, u, xn, y, dskip, (u16*)d_out);
}
```

```cpp
#include <hip/hip_runtime.h>
#include <stdint.h>

typedef unsigned short u16;
typedef unsigned int u32;
typedef unsigned long long u64;
typedef u32 v4u __attribute__((ext_vector_type(4)));
typedef u32 v2u __attribute__((ext_vector_type(2)));
typedef float v4f __attribute__((ext_vector_type(4)));
typedef float v2f __attribute__((ext_vector_type(2)));
typedef short v8s __attribute__((ext_vector_type(8)));
typedef short v4s __attribute__((ext_vector_type(4)));
typedef __bf16 v8b __attribute__((ext_vector_type(8)));
typedef __bf16 v2b __attribute__((ext_vector_type(2)));
typedef _Float16 v8h __attribute__((ext_vector_type(8)));
typedef _Float16 v2h __attribute__((ext_vector_type(2)));
#define LAS __attribute__((address_space(3)))
#define GAS __attribute__((address_space(1)))
#define RLX_AGENT __ATOMIC_RELAXED, __HIP_MEMORY_SCOPE_AGENT
#define DEVI __device__ __forceinline__

#define STAMPS 0

constexpr int L = 2048, D = 512, NS = 128, F = 1792;
constexpr int WS_E = 0, WS_FA = 1024, WS_FH = 2048, WS_FS = 3072, WS_TMO = 4096, WS_STAT = 8192;
constexpr int WS_XNB = 65536, WS_SB = WS_XNB + (2 << 20), WS_H = WS_SB + (2 << 20), WS_END = WS_H + L * F * 2;
constexpr int WS_STAMP = 16 << 20;
constexpr int SLOT = 32768;
constexpr int AUX = 3 * SLOT;
constexpr int L_TAB = AUX;
constexpr int L_LN = 160 * 1024 - 8 * 1088;
constexpr unsigned kSpin = 1u << 20;

__shared__ __attribute__((aligned(16))) char smem[163840];
#define LP(T, off) ((LAS T*)((LAS char*)smem + (off)))

DEVI float bf2f(u32 v) { return __uint_as_float(v << 16); }
DEVI float bfhi(u32 v) { return __uint_as_float(v & 0xffff0000u); }
DEVI u32 pkbf(float a, float b) { v2f v = {a, b}; return __builtin_bit_cast(u32, __builtin_convertvector(v, v2b)); }
DEVI u32 pkh(float a, float b) { v2f v = {a, b}; return __builtin_bit_cast(u32, __builtin_convertvector(v, v2h)); }
DEVI float rbf(float a) { return bf2f(pkbf(a, a) & 0xffffu); }
DEVI float silu(float v) { return v * __builtin_amdgcn_rcpf(1.0f + __builtin_amdgcn_exp2f(v * -1.44269504089f)); }
DEVI void bar() { asm volatile("s_waitcnt lgkmcnt(0)\n\ts_barrier" ::: "memory"); }
template <int N> DEVI void waitv() { asm volatile("s_waitcnt vmcnt(%0)" ::"n"(N) : "memory"); }
DEVI void glds(const void* sbase, u32 voff, u32 lds_dst) {
  u32 keep;
  asm volatile("s_mov_b32 %0, m0\n\ts_mov_b32 m0, %3\n\ts_nop 0\n\tglobal_load_lds_dwordx4 %1, %2\n\ts_mov_b32 m0, %0"
               : "=&s"(keep) : "v"(voff), "s"(sbase), "s"(lds_dst) : "memory");
}
DEVI v4s trread(u32 off) { return __builtin_amdgcn_ds_read_tr16_b64_v4i16(LP(v4s, off)); }
DEVI v8s cat(v4s a, v4s b) { return __builtin_shufflevector(a, b, 0, 1, 2, 3, 4, 5, 6, 7); }
DEVI v4f mfma_bf(v8s a, v8s b, v4f c) {
  return __builtin_amdgcn_mfma_f32_16x16x32_bf16(__builtin_bit_cast(v8b, a), __builtin_bit_cast(v8b, b), c, 0, 0, 0);
}
DEVI v4f mfma_h(v8s a, v8s b, v4f c) {
  return __builtin_amdgcn_mfma_f32_16x16x32_f16(__builtin_bit_cast(v8h, a), __builtin_bit_cast(v8h, b), c, 0, 0, 0);
}
DEVI float wsum(float v) {
#pragma unroll
  for (int m = 32; m >= 1; m >>= 1) v += __shfl_xor(v, m);
  return v;
}
DEVI float wmax(float v) {
#pragma unroll
  for (int m = 32; m >= 1; m >>= 1) v = fmaxf(v, __shfl_xor(v, m));
  return v;
}
struct cf { float r, i; };
DEVI cf cmul(cf a, cf b) { return {a.r * b.r - a.i * b.i, a.r * b.i + a.i * b.r}; }
DEVI cf cexp(float ar, float ai, float p) {
  float e = __builtin_amdgcn_exp2f(ar * p);
  float th = __builtin_amdgcn_fractf(ai * p);
  return {e * __builtin_amdgcn_cosf(th), e * __builtin_amdgcn_sinf(th)};
}
#define STAMP(i)

__global__ __launch_bounds__(512, 2) void fused_kernel(
    const u16* __restrict__ x, const u16* __restrict__ logA, const u16* __restrict__ Aim,
    const u16* __restrict__ Cre, const u16* __restrict__ Cim, const u16* __restrict__ logdt,
    const u16* __restrict__ dskip, const u16* __restrict__ w1, const u16* __restrict__ w2,
    u16* __restrict__ out, char* ws) {
  const int tid = threadIdx.x, l = tid & 63, w = __builtin_amdgcn_readfirstlane(tid >> 6);
  const int b = blockIdx.x, xcd = b & 7, bi = b >> 3;
  const int g = l >> 4, c16 = l & 15;
  const __amdgpu_buffer_rsrc_t rws = __builtin_amdgcn_make_buffer_rsrc(ws, 0, WS_END, 0x00020000);
  STAMP(0);

  const int mt1 = (xcd >> 1) * 4 + (bi & 3), nt1 = (xcd & 1) * 8 + (bi >> 2);
  const int tt2 = xcd * 4 + (bi & 3), dt2 = bi >> 2;
  const int tb = xcd * 32 + bi;

  const u32 tag = (u32)__builtin_amdgcn_readfirstlane(__hip_atomic_load((GAS u32*)(ws + WS_E), RLX_AGENT)) + 1u;
  const v4u xr = *(const v4u*)(x + (tb * 8 + w) * D + l * 8);
  const int kch = w >> 2, kmode = ((w >> 1) & 1) * 64 + l, krole = w & 1;
  const int kd = tb * 2 + kch;
  const u32 pA = logA[kd * NS + kmode], pI = Aim[kd * NS + kmode], pR = Cre[kd * NS + kmode], pC = Cim[kd * NS + kmode];
  const u32 pR2 = Cre[kd * NS + (kmode ^ 64)], pC2 = Cim[kd * NS + (kmode ^ 64)];
  const u32 pdt = logdt[kd];

  const u32 voffX = (u32)((8 * w + (l >> 3)) * 1024 + (((l & 7) ^ ((4 * (w & 1) + g) & 7)) * 16));
  const u32 voffW = (u32)((4 * w + g) * 3584 + ((c16 ^ ((g | (((w >> 1) & 1) << 2)) << 1)) * 16));
  const char* sX = (const char*)x + mt1 * 128 * 1024;
  const char* sW = (const char*)w1 + ((224 * nt1) & ~63);
  auto stage1 = [&](int c, int slot) {
    u32 base = (u32)(slot * SLOT + w * 1024);
    glds(sX + c * 128, voffX, base);
    glds(sX + c * 128 + 65536, voffX, base + 8192);
    glds(sW + c * 64 * 3584, voffW, base + 16384);
    glds(sW + (c * 64 + 32) * 3584, voffW, base + 16384 + 8192);
  };
  stage1(0, 0); stage1(1, 1); stage1(2, 2);

  {
    float v[8];
#pragma unroll
    for (int j = 0; j < 4; ++j) { v[2 * j] = bf2f(xr[j]); v[2 * j + 1] = bfhi(xr[j]); }
    float s = 0.f;
#pragma unroll
    for (int j = 0; j < 8; ++j) s += v[j];
    const float mean = wsum(s) * (1.0f / D);
    float q = 0.f;
#pragma unroll
    for (int j = 0; j < 8; ++j) { float t = v[j] - mean; q += t * t; }
    const float var = rbf(wsum(q) * (1.0f / D));
    const float mb = rbf(mean);
    const float den = rbf(__builtin_amdgcn_sqrtf(rbf(var + 1e-5f)));
    const float rinv = 1.0f / den;
    v4u o;
#pragma unroll
    for (int j = 0; j < 4; ++j) {
      u32 t = pkbf(v[2 * j] - mb, v[2 * j + 1] - mb);
      u32 n = pkbf(bf2f(t) * rinv, bfhi(t) * rinv);
      o[j] = pkh(bf2f(n), bfhi(n));
    }
    *LP(v4u, L_LN + w * 1088 + l * 16) = o;
    if (l == 0) {
      u64 gr = ((u64)tag << 32) | (u64)((__float_as_uint(mb) >> 16) | (__float_as_uint(den) & 0xffff0000u));
      __hip_atomic_store((GAS u64*)(ws + WS_STAT) + (tb * 8 + w), gr, RLX_AGENT);
    }
    bar();
    const u32 ta = (u32)(L_LN + (c16 >> 2) * 1088 + (tid - c16) * 2 + (l & 3) * 8);
    v8s tv = cat(trread(ta), trread(ta + 4 * 1088));
    __builtin_amdgcn_raw_buffer_store_b128(__builtin_bit_cast(v4u, tv), rws, WS_XNB + tb * 8192 + tid * 16, 0, 16);
    bar();
  }
  STAMP(1);
  float unscale;
  {
    const float dt = rbf(__expf(bf2f(pdt)));
    const float ar = -rbf(__expf(bf2f(pA))) * dt * 1.44269504089f;
    const float ai = bf2f(pI) * dt * 0.15915494309f;
    float cr = bf2f(pR), ci = bf2f(pC);
    float mx = wmax(fmaxf(fmaxf(fabsf(cr), fabsf(ci)), fmaxf(fabsf(bf2f(pR2)), fabsf(bf2f(pC2)))));
    int ex = (int)((__float_as_uint(mx) >> 23) & 0xff);
    ex = ex < 8 ? 8 : (ex > 250 ? 250 : ex);
    const float sc = __uint_as_float((u32)(260 - ex) << 23);
    unscale = __uint_as_float((u32)(ex - 6) << 23) * 2.0f * dt;
    const cf C = {cr * sc, ci * sc};
    const u32 sw = (u32)(((kmode >> 1) & 1) | ((kmode & 1) << 1) | (kmode & 4));
    const u32 tbase = (u32)(L_TAB + kch * 32768 + kmode * 256);
    auto put8 = [&](int chunk, const cf* v, bool neg) {
      v4u re, im;
#pragma unroll
      for (int j = 0; j < 4; ++j) {
        re[j] = pkh(v[2 * j].r, v[2 * j + 1].r);
        im[j] = neg ? pkh(-v[2 * j].i, -v[2 * j + 1].i) : pkh(v[2 * j].i, v[2 * j + 1].i);
      }
      u32 a = tbase + (((u32)chunk ^ sw) * 16);
      *LP(v4u, a) = re;
      *LP(v4u, a + 128) = im;
    };
    const cf W = cexp(ar, ai, 128.f);
    if (krole) {
      const cf z = cexp(ar, ai, 1.f);
      cf a0[8], a1[8], a2[8], a3[8];
      a0[0] = C;
#pragma unroll
      for (int e = 1; e < 8; ++e) a0[e] = cmul(a0[e - 1], z);
      put8(0, a0, true);
#pragma unroll
      for (int e = 0; e < 8; ++e) a1[e] = cmul(a0[e], W);
      put8(1, a1, true);
#pragma unroll
      for (int e = 0; e < 4; ++e) { a2[e] = cmul(a1[2 * e], W); a2[4 + e] = cmul(a2[e], W); }
      put8(2, a2, true);
      a3[0] = cmul(a2[4], W); a3[1] = cmul(a2[6], W);
#pragma unroll
      for (int e = 2; e < 8; ++e) a3[e] = cmul(a3[e - 2], W);
      put8(3, a3, true);
    } else {
      const cf z8 = cexp(ar, ai, 8.f);
      cf y0[8], y1[8], a4[8], a5[8];
      y0[0] = {1.f, 0.f}; y0[1] = z8;
#pragma unroll
      for (int e = 2; e < 8; ++e) y0[e] = cmul(y0[e - 1], z8);
      put8(6, y0, false);
      const cf z64 = cmul(y0[4], y0[4]);
#pragma unroll
      for (int e = 0; e < 8; ++e) y1[e] = cmul(y0[e], z64);
      put8(7, y1, false);
      a4[0] = cmul(C, cexp(ar, ai, 1024.f));
#pragma unroll
      for (int e = 1; e < 8; ++e) a4[e] = cmul(a4[e - 1], W);
      put8(4, a4, true);
      a5[0] = cmul(a4[7], W);
#pragma unroll
      for (int e = 1; e < 8; ++e) a5[e] = {0.f, 0.f};
      put8(5, a5, true);
    }
  }
  bar();
  constexpr int L_KD = AUX;
  constexpr int L_US = AUX + 4096;
  constexpr int L_CS = AUX + 4096 + 64;
  {
    v4f kacc = {0.f, 0.f, 0.f, 0.f};
    const int ch = w / 3, tile = w - ch * 3;
    if (w < 6) {
      const int q = c16 >> 2, p = l & 3;
#pragma unroll
      for (int s = 0; s < 8; ++s) {
        v4s a[2], bb[2];
#pragma unroll
        for (int t = 0; t < 2; ++t) {
          const int row = 8 * g + 4 * t + q, n = row >> 1;
          const u32 sw = (u32)(((n >> 1) & 1) | ((n & 1) << 1) | (n & 4));
          const u32 rb = (u32)(L_TAB + ch * 32768 + (32 * s + row) * 128 + (p & 1) * 8);
          a[t] = trread(rb + (((u32)(2 * tile + (p >> 1)) ^ sw) * 16));
          bb[t] = trread(rb + (((u32)(6 + (p >> 1)) ^ sw) * 16));
        }
        kacc = mfma_h(cat(a[0], a[1]), cat(bb[0], bb[1]), kacc);
      }
    }
    bar();
    if (w < 6) {
#pragma unroll
      for (int r = 0; r < 4; ++r) {
        int idx;
        if (tile == 0) idx = 128 * (g >> 1) + 8 * c16 + 4 * (g & 1) + r;
        else if (tile == 1) idx = g < 2 ? 256 + 64 * g + r + 4 * c16 : 384 + 32 * (2 * (g - 2) + (r >> 1)) + (r & 1) + 2 * c16;
        else idx = (4 * g + r) <= 8 ? 512 + 16 * (4 * g + r) + c16 : 660;
        *LP(u16, L_KD + ch * 1408 + (idx + 4) * 2) = (u16)pkh(kacc[r], kacc[r]);
      }
    }
    if (w >= 6 && l == 0) *LP(v2u, L_KD + (w - 6) * 1408) = (v2u){0u, 0u};
    if ((w & 3) == 0 && l == 0) *LP(float, L_US + kch * 4) = unscale;
  }
  STAMP(2);
  const int tq = w & 3, fh = w >> 2;
  v4f acc1[2][4], accs[4];
#pragma unroll
  for (int i = 0; i < 4; ++i) { acc1[0][i] = (v4f){0.f, 0.f, 0.f, 0.f}; acc1[1][i] = acc1[0][i]; accs[i] = acc1[0][i]; }
  {
    const u32 xb = (u32)((32 * tq + c16) * 128 + ((g ^ (c16 >> 1)) * 16));
    const int q = c16 >> 2, p = l & 3;
    const u32 hh = (u32)(q | ((g & 1) << 2));
    const u32 d32 = (u32)(nt1 & 1);
    u32 wb[4];
#pragma unroll
    for (int i = 0; i < 4; ++i) wb[i] = (u32)(16384 + (8 * g + q) * 256 + (((d32 + 4 * fh + i) & 7) ^ hh) * 32 + p * 8);
    const v8s ones = {0x3f80, 0x3f80, 0x3f80, 0x3f80, 0x3f80, 0x3f80, 0x3f80, 0x3f80};
    int slot = 0;
    for (int c = 0; c < 8; ++c) {
      if (c == 0) waitv<0>();
      else if (c < 7) waitv<4>();
      else waitv<0>();
      bar();
      if (c == 0 && tid == 0) __hip_atomic_store((GAS u32*)(ws + WS_FA) + tb, tag, RLX_AGENT);
      if (c >= 1 && c < 6) stage1(c + 2, slot == 0 ? 2 : slot - 1);
      const u32 sb = (u32)(slot * SLOT);
      const bool docs = (c >> 1) == tq;
#pragma unroll
      for (int s = 0; s < 2; ++s) {
        v8s bx[2], aw[4];
#pragma unroll
        for (int bt = 0; bt < 2; ++bt) bx[bt] = *LP(v8s, sb + ((xb ^ (s * 64)) + bt * 2048));
#pragma unroll
        for (int i = 0; i < 3; ++i) aw[i] = cat(trread(sb + wb[i] + s * 8192), trread(sb + wb[i] + s * 8192 + 1024));
#pragma unroll
        for (int i = 0; i < 3; ++i) {
          acc1[0][i] = mfma_bf(aw[i], bx[0], acc1[0][i]);
          acc1[1][i] = mfma_bf(aw[i], bx[1], acc1[1][i]);
        }
        if (docs) {
#pragma unroll
          for (int i = 0; i < 3; ++i) accs[i] = mfma_bf(aw[i], ones, accs[i]);
        }
        if (fh == 0) {
          aw[3] = cat(trread(sb + wb[3] + s * 8192), trread(sb + wb[3] + s * 8192 + 1024));
          acc1[0][3] = mfma_bf(aw[3], bx[0], acc1[0][3]);
          acc1[1][3] = mfma_bf(aw[3], bx[1], acc1[1][3]);
          if (docs) accs[3] = mfma_bf(aw[3], ones, accs[3]);
        }
      }
      slot = slot == 2 ? 0 : slot + 1;
    }
  }
  STAMP(3);
  {
    if (c16 == 0) {
#pragma unroll
      for (int i = 0; i < 4; ++i)
        if (i < 3 || fh == 0) *LP(v4f, L_CS + tq * 448 + ((4 * fh + i) * 16 + 4 * g) * 4) = accs[i];
    }
    float mrow[2], rrow[2];
    {
      u32 spins = 0;
      for (;;) {
        u64 g0 = __hip_atomic_load((GAS u64*)(ws + WS_STAT) + (mt1 * 128 + 32 * tq + c16), RLX_AGENT);
        u64 g1 = __hip_atomic_load((GAS u64*)(ws + WS_STAT) + (mt1 * 128 + 32 * tq + 16 + c16), RLX_AGENT);
        mrow[0] = bf2f((u32)g0); rrow[0] = 1.0f / bfhi((u32)g0);
        mrow[1] = bf2f((u32)g1); rrow[1] = 1.0f / bfhi((u32)g1);
        if (__all((u32)(g0 >> 32) == tag && (u32)(g1 >> 32) == tag)) break;
        if (++spins > kSpin) break;
        __builtin_amdgcn_s_sleep(1);
      }
    }
    bar();
    u32 hp[2][4][2];
#pragma unroll
    for (int i = 0; i < 4; ++i) {
      v4f cs = {0.f, 0.f, 0.f, 0.f};
      if (i < 3 || fh == 0) {
#pragma unroll
        for (int k = 0; k < 4; ++k) cs += *LP(v4f, L_CS + k * 448 + ((4 * fh + i) * 16 + 4 * g) * 4);
      }
#pragma unroll
      for (int bt = 0; bt < 2; ++bt) {
        float hv[4];
#pragma unroll
        for (int r = 0; r < 4; ++r) hv[r] = silu((acc1[bt][i][r] - mrow[bt] * cs[r]) * rrow[bt]);
        hp[bt][i][0] = pkbf(hv[0], hv[1]);
        hp[bt][i][1] = pkbf(hv[2], hv[3]);
      }
    }
#pragma unroll
    for (int bt = 0; bt < 2; ++bt) {
      const int T = mt1 * 8 + 2 * tq + bt;
      const int kb = (T * 56 + 7 * (nt1 >> 1)) * 1024;
      const int j0 = 3 * (nt1 & 1) + 2 * fh;
      __builtin_amdgcn_raw_buffer_store_b128((v4u){hp[bt][0][0], hp[bt][0][1], hp[bt][1][0], hp[bt][1][1]}, rws,
                                             WS_H + kb + j0 * 1024 + l * 16, 0, 16);
      if (fh == 0)
        __builtin_amdgcn_raw_buffer_store_b128((v4u){hp[bt][2][0], hp[bt][2][1], hp[bt][3][0], hp[bt][3][1]}, rws,
                                               WS_H + kb + (j0 + 1) * 1024 + l * 16, 0, 16);
      else
        __builtin_amdgcn_raw_buffer_store_b64((v2u){hp[bt][2][0], hp[bt][2][1]}, rws,
                                              WS_H + kb + 6 * 1024 + (nt1 & 1) * 512 + l * 8, 0, 16);
    }
    waitv<0>();
    bar();
    if (tid == 0) __hip_atomic_store((GAS u32*)(ws + WS_FH) + mt1 * 16 + nt1, tag, RLX_AGENT);
  }
  STAMP(4);
  v8s wf[7][4];
  {
    const u32 voff2 = (u32)((l >> 3) * 1024 + (((l & 7) ^ ((g & 3) << 1)) * 16));
    const char* s2 = (const char*)w2 + (224 * w) * 1024 + dt2 * 128;
    const u32 st = (u32)(w * 12288);
    const int q = c16 >> 2, p = l & 3;
    u32 ra[4];
#pragma unroll
    for (int dj = 0; dj < 4; ++dj) ra[dj] = st + (u32)((4 * g + q) * 128 + ((dj ^ ((2 * g + (q >> 1)) & 3)) * 32) + p * 8);
#pragma unroll
    for (int rnd = 0; rnd < 3; ++rnd) {
#pragma unroll
      for (int jj = 0; jj < 3; ++jj) {
        const int j = rnd * 3 + jj;
        if (j < 7) {
#pragma unroll
          for (int pc = 0; pc < 4; ++pc) {
            const int row = j < 6 ? (j / 3) * 112 + (j % 3) * 32 + pc * 8 : 96 + (pc >> 1) * 112 + (pc & 1) * 8;
            glds(s2 + row * 1024, voff2, st + jj * 4096 + pc * 1024);
          }
        }
      }
      waitv<0>();
#pragma unroll
      for (int jj = 0; jj < 3; ++jj) {
        const int j = rnd * 3 + jj;
        if (j < 7) {
#pragma unroll
          for (int dj = 0; dj < 4; ++dj) wf[j][dj] = cat(trread(ra[dj] + jj * 4096), trread(ra[dj] + jj * 4096 + 2048));
        }
      }
      asm volatile("s_waitcnt lgkmcnt(0)" ::: "memory");
    }
  }
  STAMP(5);
  constexpr int L_R = AUX + 8192;
  constexpr int L_U = L_R + 8 * 4160;
  constexpr int L_P = 0;
  {
    if (w == 0) {
      u32 spins = 0;
      for (;;) {
        v4u f = __builtin_amdgcn_raw_buffer_load_b128(rws, WS_FA + l * 16, 0, 16);
        if (__all(f[0] == tag && f[1] == tag && f[2] == tag && f[3] == tag)) break;
        if (++spins > kSpin) break;
        __builtin_amdgcn_s_sleep(1);
      }
      if (b == 0 && l == 0) __hip_atomic_store((GAS u32*)(ws + WS_E), tag, RLX_AGENT);
    }
    bar();
    const v4u uv = __builtin_amdgcn_raw_buffer_load_b128(rws, WS_XNB + (tid >> 1) * 8192 + (tb * 2 + (tid & 1)) * 16, 0, 16);
    {
      const int ch = tid >> 8, i = tid & 255;
      const int lagtop = 2047 - 8 * i;
      u32 kv[11];
#pragma unroll
      for (int e = 0; e < 11; ++e) {
        const int lag = lagtop - e;
        const int idx = lagtop >= 160 ? (int)(pkbf((float)lag, (float)lag) & 0xffffu) - 17024 : lag;
        kv[e] = *LP(u16, L_KD + ch * 1408 + (idx + 4) * 2);
      }
#pragma unroll
      for (int sg = 0; sg < 4; ++sg) {
        v4u o;
#pragma unroll
        for (int j = 0; j < 4; ++j) o[j] = kv[3 - sg + 2 * j] | (kv[3 - sg + 2 * j + 1] << 16);
        *LP(v4u, L_R + (ch * 4 + sg) * 4160 + i * 16) = o;
      }
      if (tid < 16) *LP(v4u, L_R + (tid >> 1) * 4160 + 4096 + (tid & 1) * 16) = (v4u){0u, 0u, 0u, 0u};
      if (tid < 64) *LP(v4u, L_U + (tid >> 5) * 4608 + (tid & 31) * 16) = (v4u){0u, 0u, 0u, 0u};
    }
    *LP(v4u, L_U + (tid & 1) * 4608 + 512 + (tid >> 1) * 16) = uv;
    bar();
    STAMP(6);
    const int ch = w >> 2, qh = (w >> 1) & 1, ng = w & 1;
    v4f ya[4];
#pragma unroll
    for (int i = 0; i < 4; ++i) ya[i] = (v4f){0.f, 0.f, 0.f, 0.f};
    const u32 fb = (u32)(L_R + (ch * 4 + (c16 & 3)) * 4160 + (2028 - 32 * 56 + 8 * g - 4 * (c16 >> 2)) * 2);
    const u32 gb = (u32)(L_U + ch * 4608 + 512 + (16 * c16 - 16 + 8 * g) * 2);
    for (int qq = 0; qq < 4; ++qq) {
      const int q0 = qh * 4 + qq;
      v8s Ff[8], Gf[8];
#pragma unroll
      for (int i = 0; i < 8; ++i) {
        const u32 a = fb - q0 * 64 + (7 - i) * 512;
        Ff[i] = cat(*LP(v4s, a), *LP(v4s, a + 8));
        Gf[i] = *LP(v8s, gb - q0 * 64 + i * 512);
      }
      if (ng == 0) {
        ya[0] = mfma_h(Ff[0], Gf[0], ya[0]);
#pragma unroll
        for (int i = 0; i <= 3; ++i) ya[1] = mfma_h(Ff[i], Gf[3 - i], ya[1]);
#pragma unroll
        for (int i = 0; i <= 4; ++i) ya[2] = mfma_h(Ff[i], Gf[4 - i], ya[2]);
#pragma unroll
        for (int i = 0; i <= 7; ++i) ya[3] = mfma_h(Ff[i], Gf[7 - i], ya[3]);
      } else {
#pragma unroll
        for (int i = 0; i <= 1; ++i) ya[0] = mfma_h(Ff[i], Gf[1 - i], ya[0]);
#pragma unroll
        for (int i = 0; i <= 2; ++i) ya[1] = mfma_h(Ff[i], Gf[2 - i], ya[1]);
#pragma unroll
        for (int i = 0; i <= 5; ++i) ya[2] = mfma_h(Ff[i], Gf[5 - i], ya[2]);
#pragma unroll
        for (int i = 0; i <= 6; ++i) ya[3] = mfma_h(Ff[i], Gf[6 - i], ya[3]);
      }
    }
#pragma unroll
    for (int i = 0; i < 4; ++i) {
      const int n = ng == 0 ? (i == 0 ? 0 : i == 1 ? 3 : i == 2 ? 4 : 7) : (i == 0 ? 1 : i == 1 ? 2 : i == 2 ? 5 : 6);
      *LP(v4f, L_P + ((ch * 2 + qh) * 8 + n) * 1280 + (g * 20 + c16) * 16) = ya[i];
    }
    bar();
    {
      const int och = tid >> 8, i = tid & 255;
      const int n = i >> 5, cc = (i >> 1) & 15, hf = i & 1;
      const u32 pa = (u32)(L_P + (och * 16 + n) * 1280 + (hf * 40 + cc) * 16);
      const v4f y0 = *LP(v4f, pa) + *LP(v4f, pa + 8 * 1280);
      const v4f y1 = *LP(v4f, pa + 320) + *LP(v4f, pa + 8 * 1280 + 320);
      const v8h u8 = *LP(v8h, L_U + och * 4608 + 512 + i * 16);
      const float us = *LP(float, L_US + och * 4);
      const float ds = bf2f(dskip[tb * 2 + och]);
      v4u o;
#pragma unroll
      for (int j = 0; j < 4; ++j) {
        const float ya0 = j < 2 ? y0[2 * j] : y1[2 * j - 4], ya1 = j < 2 ? y0[2 * j + 1] : y1[2 * j - 3];
        const u32 yb = pkbf(ya0 * us, ya1 * us);
        const u32 sk = pkbf((float)u8[2 * j] * ds, (float)u8[2 * j + 1] * ds);
        o[j] = pkbf(bf2f(yb) + bf2f(sk), bfhi(yb) + bfhi(sk));
      }
      __builtin_amdgcn_raw_buffer_store_b128(o, rws, WS_SB + ((i >> 3) * 512 + tb * 2 + och) * 128 + (i & 7) * 16, 0, 16);
    }
    waitv<0>();
    bar();
    if (tid == 0) __hip_atomic_store((GAS u32*)(ws + WS_FS) + tb, tag, RLX_AGENT);
  }
  STAMP(7);
  {
    {
      u32 spins = 0;
      for (;;) {
        u64 f = __hip_atomic_load((GAS u64*)(ws + WS_FH) + (tt2 >> 1) * 8 + w, RLX_AGENT);
        if (__all((u32)f == tag && (u32)(f >> 32) == tag)) break;
        if (++spins > kSpin) break;
        __builtin_amdgcn_s_sleep(1);
      }
      __builtin_amdgcn_fence(__ATOMIC_ACQUIRE, "wavefront");
    }
    STAMP(8);
    constexpr int L_Q = 0;
    constexpr int L_S = AUX + 49152;
#pragma unroll
    for (int ti = 0; ti < 4; ++ti) {
      v4u hf[7];
      const int hb = WS_H + ((tt2 * 4 + ti) * 56 + 7 * w) * 1024;
#pragma unroll
      for (int j = 0; j < 6; ++j) hf[j] = __builtin_amdgcn_raw_buffer_load_b128(rws, hb + j * 1024 + l * 16, 0, 16);
      {
        v2u h0 = __builtin_amdgcn_raw_buffer_load_b64(rws, hb + 6144 + l * 8, 0, 16);
        v2u h1 = __builtin_amdgcn_raw_buffer_load_b64(rws, hb + 6144 + 512 + l * 8, 0, 16);
        hf[6] = (v4u){h0[0], h0[1], h1[0], h1[1]};
      }
      v4f a2[4];
#pragma unroll
      for (int dj = 0; dj < 4; ++dj) a2[dj] = (v4f){0.f, 0.f, 0.f, 0.f};
#pragma unroll
      for (int j = 0; j < 7; ++j)
#pragma unroll
        for (int dj = 0; dj < 4; ++dj) a2[dj] = mfma_bf(wf[j][dj], __builtin_bit_cast(v8s, hf[j]), a2[dj]);
#pragma unroll
      for (int dj = 0; dj < 4; ++dj)
        *LP(v4f, L_Q + (ti * 8 + w) * 4096 + c16 * 256 + (((dj * 4 + g) ^ (c16 & 7)) * 16)) = a2[dj];
    }
    STAMP(9);
    if (w == 0) {
      u32 spins = 0;
      for (;;) {
        u32 f = __hip_atomic_load((GAS u32*)(ws + WS_FS) + dt2 * 32 + (l & 31), RLX_AGENT);
        if (__all(f == tag)) break;
        if (++spins > kSpin) break;
        __builtin_amdgcn_s_sleep(1);
      }
    }
    bar();
    {
      const v4u sv = __builtin_amdgcn_raw_buffer_load_b128(rws, WS_SB + (tt2 * 512 + dt2 * 64) * 128 + tid * 16, 0, 16);
      const int d = tid >> 3;
      *LP(v4u, L_S + d * 128 + (((tid & 7) ^ ((d >> 2) & 7)) * 16)) = sv;
    }
    bar();
#pragma unroll
    for (int rr = 0; rr < 2; ++rr) {
      const int ti = rr * 2 + (w >> 2), t = (w & 3) * 4 + g, tl = ti * 16 + t;
      v4f sum = {0.f, 0.f, 0.f, 0.f};
#pragma unroll
      for (int k = 0; k < 8; ++k) sum += *LP(v4f, L_Q + (ti * 8 + k) * 4096 + t * 256 + ((c16 ^ (t & 7)) * 16));
      const int go = (tt2 * 64 + tl) * D + dt2 * 64 + c16 * 4;
      const v2u xv = *(const v2u*)(x + go);
      float sv[4];
#pragma unroll
      for (int r = 0; r < 4; ++r)
        sv[r] = bf2f(*LP(u16, L_S + (c16 * 4 + r) * 128 + (((tl >> 3) ^ (c16 & 7)) * 16) + (tl & 7) * 2));
      const u32 f01 = pkbf(sum[0], sum[1]), f23 = pkbf(sum[2], sum[3]);
      const u32 g01 = pkbf(silu(bf2f(f01)), silu(bfhi(f01))), g23 = pkbf(silu(bf2f(f23)), silu(bfhi(f23)));
      const u32 p01 = pkbf(sv[0] * bf2f(g01), sv[1] * bfhi(g01)), p23 = pkbf(sv[2] * bf2f(g23), sv[3] * bfhi(g23));
      v2u o;
      o[0] = pkbf(bf2f(xv[0]) + bf2f(p01), bfhi(xv[0]) + bfhi(p01));
      o[1] = pkbf(bf2f(xv[1]) + bf2f(p23), bfhi(xv[1]) + bfhi(p23));
      *(v2u*)(out + go) = o;
    }
  }
  STAMP(10);
}

extern "C" void kernel_launch(void* const* d_in, const int* in_sizes, int n_in, void* d_out, int out_size,
                              void* d_ws, size_t ws_size, hipStream_t stream) {
  fused_kernel<<<256, 512, 0, stream>>>((const u16*)d_in[0], (const u16*)d_in[1], (const u16*)d_in[2],
                                        (const u16*)d_in[3], (const u16*)d_in[4], (const u16*)d_in[5],
                                        (const u16*)d_in[6], (const u16*)d_in[8], (const u16*)d_in[9],
                                        (u16*)d_out, (char*)d_ws);
}
```

```cpp
#include <hip/hip_runtime.h>
#include <stdint.h>

typedef unsigned short u16;
typedef unsigned int u32;
typedef unsigned long long u64;
typedef u32 v4u __attribute__((ext_vector_type(4)));
typedef u32 v2u __attribute__((ext_vector_type(2)));
typedef float v4f __attribute__((ext_vector_type(4)));
typedef float v2f __attribute__((ext_vector_type(2)));
typedef float v16f __attribute__((ext_vector_type(16)));
typedef short v8s __attribute__((ext_vector_type(8)));
typedef short v4s __attribute__((ext_vector_type(4)));
typedef __bf16 v8b __attribute__((ext_vector_type(8)));
typedef __bf16 v2b __attribute__((ext_vector_type(2)));
typedef _Float16 v8h __attribute__((ext_vector_type(8)));
typedef _Float16 v4h __attribute__((ext_vector_type(4)));
typedef _Float16 v2h __attribute__((ext_vector_type(2)));
#define LAS __attribute__((address_space(3)))
#define GAS __attribute__((address_space(1)))
#define DEVI __device__ __forceinline__
#if defined(__HIP_DEVICE_COMPILE__)
#define NOPK __attribute__((target("no-packed-fp32-ops")))
#else
#define NOPK
#endif

#define STAMPS 0

constexpr int L = 2048, D = 512, NS = 128, F = 1792;
constexpr int WS_E = 0, WS_FA = 1024, WS_STAT = 8192, WS_CS = 40960;
constexpr int WS_XNB = 65536, WS_SG = WS_XNB + (2 << 20), WS_H = WS_SG + (4 << 20), WS_FH = WS_H + L * F * 2;
constexpr int WS_STAMP = 16 << 20;
constexpr int SLOT = 32768, AUX = 3 * SLOT;
constexpr int L_TA = AUX, L_TB = AUX + 32768, TB_P = 112;
constexpr int L_LN = L_TB;
constexpr int L_KD = AUX + 61440, KD_P = 1488, L_US = L_KD + 2976, L_CSP = L_KD + 3072;
constexpr int L_R = AUX, L_U = AUX + 33280, L_S = AUX + 42496;
constexpr unsigned kSpin = 1u << 20;

__shared__ __attribute__((aligned(16))) char smem[163840];
#define LP(T, off) ((LAS T*)((LAS char*)smem + (off)))
#define PIN(x) asm volatile("" : "+v"(x))
#define OPQ(x) asm("" : "+v"(x))
#define SB0() __builtin_amdgcn_sched_barrier(0)

DEVI float u2f(u32 v) { return __builtin_bit_cast(float, v); }
DEVI u32 f2u(float v) { return __builtin_bit_cast(u32, v); }
DEVI bool wall(bool p) { return __builtin_amdgcn_ballot_w64(!p) == 0; }
DEVI float bf2f(u32 v) { return u2f(v << 16); }
DEVI float bfhi(u32 v) { return u2f(v & 0xffff0000u); }
DEVI u32 pkbf(float a, float b) { v2f v = {a, b}; return __builtin_bit_cast(u32, __builtin_convertvector(v, v2b)); }
DEVI u32 pkh(float a, float b) { v2f v = {a, b}; return __builtin_bit_cast(u32, __builtin_convertvector(v, v2h)); }
DEVI float rbf(float a) { return bf2f(pkbf(a, a)); }
DEVI float silu(float v) { return v * __builtin_amdgcn_rcpf(1.0f + __builtin_amdgcn_exp2f(v * -1.44269504089f)); }
DEVI void waitl() { asm volatile("" ::: "memory"); __builtin_amdgcn_s_waitcnt(0xC07F); asm volatile("" ::: "memory"); }
DEVI void bar() { waitl(); asm volatile("s_barrier" ::: "memory"); }
template <int N> DEVI void waitv() { asm volatile("s_waitcnt vmcnt(%0)" ::"n"(N) : "memory"); }
typedef __amdgpu_buffer_rsrc_t rsrc_t;
DEVI rsrc_t mkrs(const void* p, u32 n) { return __builtin_amdgcn_make_buffer_rsrc((void*)p, 0, n, 0x00020000); }
template <int O> DEVI void glds2(rsrc_t rs, u32 so, u32 va, u32 vb, u32 m) {
  asm volatile("s_mov_b32 m0, %4\n\ts_nop 0\n\tbuffer_load_dwordx4 %0, %2, %3 offen offset:%5 lds\n\tbuffer_load_dwordx4 %1, %2, %3 offen offset:%6 lds"
               ::"v"(va), "v"(vb), "s"(rs), "s"(so), "s"(m), "n"(O), "n"(O + 1024) : "memory");
}
template <int O> DEVI void glds1(rsrc_t rs, u32 so, u32 va, u32 m) {
  asm volatile("s_mov_b32 m0, %3\n\ts_nop 0\n\tbuffer_load_dwordx4 %0, %1, %2 offen offset:%4 lds"
               ::"v"(va), "s"(rs), "s"(so), "s"(m), "n"(O) : "memory");
}
template <int O> DEVI void bst4c(v4u d, u32 voff, rsrc_t rs, u32 so) { asm volatile("buffer_store_dwordx4 %0, %1, %2, %3 offen offset:%4 sc1\n\ts_nop 1" ::"v"(d), "v"(voff), "s"(rs), "s"(so), "n"(O) : "memory"); }
template <int O> DEVI void bst1c(u32 d, rsrc_t rs, u32 so) { asm volatile("buffer_store_dword %0, off, %1, %2 offset:%3 sc1" ::"v"(d), "s"(rs), "s"(so), "n"(O) : "memory"); }
template <int O> DEVI void ld4(v4u& d, u32 voff, const void* sb) { asm volatile("global_load_dwordx4 %0, %1, %2 offset:%3" : "=v"(d) : "v"(voff), "s"(sb), "n"(O) : "memory"); }
template <int O> DEVI void ld4c(v4u& d, u32 voff, const void* sb) { asm volatile("global_load_dwordx4 %0, %1, %2 offset:%3 sc1" : "=v"(d) : "v"(voff), "s"(sb), "n"(O) : "memory"); }
template <int O> DEVI void ld2(v2u& d, u32 voff, const void* sb) { asm volatile("global_load_dwordx2 %0, %1, %2 offset:%3" : "=v"(d) : "v"(voff), "s"(sb), "n"(O) : "memory"); }
template <int O> DEVI void ld2c(v2u& d, u32 voff, const void* sb) { asm volatile("global_load_dwordx2 %0, %1, %2 offset:%3 sc1" : "=v"(d) : "v"(voff), "s"(sb), "n"(O) : "memory"); }
template <int O> DEVI void ld1c(u32& d, u32 voff, const void* sb) { asm volatile("global_load_dword %0, %1, %2 offset:%3 sc1" : "=v"(d) : "v"(voff), "s"(sb), "n"(O) : "memory"); }
DEVI void ldus(u32& d, u32 voff, const void* sb) { asm volatile("global_load_ushort %0, %1, %2" : "=v"(d) : "v"(voff), "s"(sb) : "memory"); }
template <int O> DEVI void st4c(v4u d, u32 voff, void* sb) { asm volatile("global_store_dwordx4 %0, %1, %2 offset:%3 sc1\n\ts_nop 1" ::"v"(voff), "v"(d), "s"(sb), "n"(O) : "memory"); }
template <int O> DEVI void st2c(v2u d, u32 voff, void* sb) { asm volatile("global_store_dwordx2 %0, %1, %2 offset:%3 sc1" ::"v"(voff), "v"(d), "s"(sb), "n"(O) : "memory"); }
template <int O> DEVI void st1c(u32 d, u32 voff, void* sb) { asm volatile("global_store_dword %0, %1, %2 offset:%3 sc1" ::"v"(voff), "v"(d), "s"(sb), "n"(O) : "memory"); }

DEVI v4s trread(u32 off) { return __builtin_amdgcn_ds_read_tr16_b64_v4i16(LP(v4s, off)); }
DEVI v8s cat(v4s a, v4s b) { return __builtin_shufflevector(a, b, 0, 1, 2, 3, 4, 5, 6, 7); }
DEVI v4f mfma_bf(v8s a, v8s b, v4f c) {
  return __builtin_amdgcn_mfma_f32_16x16x32_bf16(__builtin_bit_cast(v8b, a), __builtin_bit_cast(v8b, b), c, 0, 0, 0);
}
DEVI v16f mfma32(v8s a, v8s b, v16f c) {
  return __builtin_amdgcn_mfma_f32_32x32x16_bf16(__builtin_bit_cast(v8b, a), __builtin_bit_cast(v8b, b), c, 0, 0, 0);
}
DEVI v4f mfma_h(v8s a, v8s b, v4f c) {
  return __builtin_amdgcn_mfma_f32_16x16x32_f16(__builtin_bit_cast(v8h, a), __builtin_bit_cast(v8h, b), c, 0, 0, 0);
}
template <int C> DEVI float dpp(float v) { return u2f((u32)__builtin_amdgcn_update_dpp(0, (int)f2u(v), C, 0xf, 0xf, true)); }
DEVI float sum16x(float v) {
  auto a = __builtin_amdgcn_permlane16_swap(f2u(v), f2u(v), false, false);
  return u2f(a[0]) + u2f(a[1]);
}
DEVI float sum32x(float v) {
  auto a = __builtin_amdgcn_permlane32_swap(f2u(v), f2u(v), false, false);
  return u2f(a[0]) + u2f(a[1]);
}
DEVI float wsum(float v) {
  v += dpp<0xB1>(v); v += dpp<0x4E>(v); v += dpp<0x141>(v); v += dpp<0x140>(v);
  return sum32x(sum16x(v));
}
template <int C> DEVI u32 dppu(u32 v) { return (u32)__builtin_amdgcn_update_dpp(0, (int)v, C, 0xf, 0xf, true); }
DEVI u32 umax(u32 a, u32 b) { return a > b ? a : b; }
DEVI u32 wmaxu(u32 v) {
  v = umax(v, dppu<0xB1>(v)); v = umax(v, dppu<0x4E>(v)); v = umax(v, dppu<0x141>(v)); v = umax(v, dppu<0x140>(v));
  auto a = __builtin_amdgcn_permlane16_swap(v, v, false, false);
  v = umax(a[0], a[1]);
  auto b = __builtin_amdgcn_permlane32_swap(v, v, false, false);
  return umax(b[0], b[1]);
}
struct cf { float r, i; };
DEVI cf cmul(cf a, cf b) { return {a.r * b.r - a.i * b.i, a.r * b.i + a.i * b.r}; }
DEVI cf cexp(float ar, float ai, float p) {
  float e = __builtin_amdgcn_exp2f(ar * p);
  float th = __builtin_amdgcn_fractf(ai * p);
  return {e * __builtin_amdgcn_cosf(th), e * __builtin_amdgcn_sinf(th)};
}
#define STAMP(i)
#define ESTAMP(i)
#define BETA(i) bar()

struct G1 {
  v16f acc[2][2];
  v8s xa[2][2], wa[2][2], xb[2][2], wb[2][2];
  u32 xo[4], wo[2];
};
DEVI void g1_read(G1& s, v8s (&xf)[2][2], v8s (&wf)[2][2], u32 sb, int half) {
#pragma unroll
  for (int k = 0; k < 2; ++k) {
    const int ks = half * 2 + k;
    const u32 xa = sb + s.xo[ks];
    xf[k][0] = *LP(v8s, xa);
    wf[k][0] = cat(trread(sb + s.wo[0] + ks * 4096), trread(sb + s.wo[0] + ks * 4096 + 1024));
    wf[k][1] = cat(trread(sb + s.wo[1] + ks * 4096), trread(sb + s.wo[1] + ks * 4096 + 1024));
    xf[k][1] = *LP(v8s, xa + 4096);
  }
}
DEVI void g1_mma(G1& s, v8s (&xf)[2][2], v8s (&wf)[2][2]) {
#pragma unroll
  for (int k = 0; k < 2; ++k) {
    s.acc[0][0] = mfma32(wf[k][0], xf[k][0], s.acc[0][0]);
    s.acc[0][1] = mfma32(wf[k][1], xf[k][0], s.acc[0][1]);
    s.acc[1][0] = mfma32(wf[k][0], xf[k][1], s.acc[1][0]);
    s.acc[1][1] = mfma32(wf[k][1], xf[k][1], s.acc[1][1]);
  }
}
DEVI void g1_sched() {
  __builtin_amdgcn_sched_group_barrier(0x8, 1, 0);
  __builtin_amdgcn_sched_group_barrier(0x2, 4, 0);
#pragma unroll
  for (int i = 0; i < 4; ++i) {
    __builtin_amdgcn_sched_group_barrier(0x100, 3, 0);
    __builtin_amdgcn_sched_group_barrier(0x8, 1, 0);
  }
  __builtin_amdgcn_sched_group_barrier(0x8, 3, 0);
}

NOPK __global__ __launch_bounds__(512, 2) void fused_kernel(
    const u16* x, const u16* logA, const u16* Aim, const u16* Cre, const u16* Cim, const u16* logdt,
    const u16* dskip, const u16* w1, const u16* w2, u16* out, char* ws) {
  asm volatile("" : "+s"(x), "+s"(logA), "+s"(Aim), "+s"(Cre), "+s"(Cim), "+s"(logdt), "+s"(dskip), "+s"(w1), "+s"(w2), "+s"(out), "+s"(ws));
  const int tid = __builtin_amdgcn_workitem_id_x(), l = tid & 63, w = __builtin_amdgcn_readfirstlane(tid >> 6);
  const int b = __builtin_amdgcn_workgroup_id_x(), xcd = b & 7, bi = b >> 3;
  const int g = l >> 4, c16 = l & 15, q4 = c16 >> 2, p4 = l & 3, v = w & 3;
  STAMP(0);

  const bool hasg1 = bi < 28;
  const int mt1 = (xcd >> 1) * 4 + (bi & 3), nt1 = (xcd & 1) * 7 + (hasg1 ? bi >> 2 : 0);
  const int tt2 = xcd * 4 + (bi & 3), dt2 = bi >> 2;
  const int tb = xcd * 32 + bi;

  u32 vo2[4], ra[4];
  const rsrc_t rs2 = mkrs((const char*)w2 + (224 * w) * 1024 + dt2 * 128, 224 * 1024);
  const u32 nrec = hasg1 ? 0x7fffffffu : 0u;
  auto init2 = [&]() __attribute__((always_inline)) {
    const int row = 16 * (g >> 1) + 4 * (g & 1) + q4;
#pragma unroll
    for (int i = 0; i < 4; ++i) {
      vo2[i] = (u32)((l >> 3) * 1024 + (((l & 7) ^ ((g & 3) << 1)) * 16) + i * 7168);
      ra[i] = (u32)(w * 4096 + row * 128 + ((i ^ ((row >> 1) & 3)) * 32) + p4 * 8);
    }
  };
  auto stage2 = [&](int j, u32 slotb) __attribute__((always_inline)) {
    glds2<0>(rs2, j * 32768, vo2[0], vo2[1], slotb + (u32)(w * 4096));
    glds2<2048>(rs2, j * 32768, vo2[2], vo2[3], slotb + (u32)(w * 4096));
  };
  v8s wf[7][4];
  auto w2read = [&](int j, u32 slotb) __attribute__((always_inline)) {
#pragma unroll
    for (int dj = 0; dj < 4; ++dj) wf[j][dj] = cat(trread(ra[dj] + slotb), trread(ra[dj] + slotb + 1024));
  };
  u32 ev, tag;

  if (w < 4) {
    v4u xr[2];
    ld4<0>(xr[0], (u32)(l * 16), (const char*)x + (tb * 8 + 2 * v) * 1024);
    ld4<1024>(xr[1], (u32)(l * 16), (const char*)x + (tb * 8 + 2 * v) * 1024);
    ld1c<0>(ev, 0u, ws);
    G1 s1;
    const int ti = v >> 1, fi = v & 1, h = l >> 5, r32 = l & 31;
#pragma unroll
    for (int i = 0; i < 4; ++i) s1.xo[i] = (u32)((64 * ti + r32) * 128 + (((2 * i + h) ^ ((r32 >> 1) & 7)) * 16));
#pragma unroll
    for (int i = 0; i < 2; ++i) s1.wo[i] = (u32)(16384 + (8 * h + q4) * 256 + (((2 * fi + i) ^ q4) * 64) + (g & 1) * 32 + p4 * 8);
#pragma unroll
    for (int i = 0; i < 16; ++i) { s1.acc[0][0][i] = 0.f; s1.acc[0][1][i] = 0.f; s1.acc[1][0][i] = 0.f; s1.acc[1][1][i] = 0.f; }
    PIN(s1.acc[0][0]); PIN(s1.acc[0][1]); PIN(s1.acc[1][0]); PIN(s1.acc[1][1]);
    init2();
    waitv<1>(); PIN(xr[0]); PIN(xr[1]);
    STAMP(1);
    {
      float e[2][8], s[2], q[2], c0[2];
#pragma unroll
      for (int rr = 0; rr < 2; ++rr) {
        c0[rr] = bf2f((u32)__builtin_amdgcn_readfirstlane(xr[rr][0]));
#pragma unroll
        for (int j = 0; j < 4; ++j) { e[rr][2 * j] = bf2f(xr[rr][j]) - c0[rr]; e[rr][2 * j + 1] = bfhi(xr[rr][j]) - c0[rr]; }
        s[rr] = ((e[rr][0] + e[rr][1]) + (e[rr][2] + e[rr][3])) + ((e[rr][4] + e[rr][5]) + (e[rr][6] + e[rr][7]));
        q[rr] = e[rr][0] * e[rr][0];
#pragma unroll
        for (int j = 1; j < 8; ++j) q[rr] += e[rr][j] * e[rr][j];
      }
      auto ps = __builtin_amdgcn_permlane32_swap(f2u(s[0]), f2u(s[1]), false, false);
      auto pq = __builtin_amdgcn_permlane32_swap(f2u(q[0]), f2u(q[1]), false, false);
      auto pm = __builtin_amdgcn_permlane16_swap(f2u(u2f(ps[0]) + u2f(ps[1])), f2u(u2f(pq[0]) + u2f(pq[1])), false, false);
      float rv = u2f(pm[0]) + u2f(pm[1]);
      rv += dpp<0xB1>(rv); rv += dpp<0x4E>(rv); rv += dpp<0x141>(rv); rv += dpp<0x140>(rv);
      rv *= (1.0f / D);
      u32 sg[2];
#pragma unroll
      for (int rr = 0; rr < 2; ++rr) {
        const float ms = u2f((u32)__builtin_amdgcn_readlane((int)f2u(rv), 32 * rr)), mq = u2f((u32)__builtin_amdgcn_readlane((int)f2u(rv), 32 * rr + 16));
        const float var = rbf(__builtin_fmaxf(mq - ms * ms, 0.f));
        const float mb = rbf(ms + c0[rr]);
        const float den = rbf(__builtin_amdgcn_sqrtf(rbf(var + 1e-5f)));
        const float rinv = __builtin_amdgcn_rcpf(den), off = (c0[rr] - mb) * rinv;
        v4u o;
#pragma unroll
        for (int j = 0; j < 4; ++j) o[j] = pkh(e[rr][2 * j] * rinv + off, e[rr][2 * j + 1] * rinv + off);
        *LP(v4u, L_LN + (2 * v + rr) * 1088 + l * 16) = o;
        sg[rr] = (f2u(mb) >> 16) | f2u(den);
      }
      waitv<0>(); PIN(ev);
      tag = (u32)__builtin_amdgcn_readfirstlane(ev) + 1u;
      if (l == 0) st4c<0>((v4u){sg[0], tag, sg[1], tag}, 0u, ws + WS_STAT + (tb * 8 + 2 * v) * 8);
    }
    BETA(0);
    g1_read(s1, s1.xa, s1.wa, 0, 0);
    {
      u32 ta = (u32)(L_LN + q4 * 1088 + v * 256 + g * 32 + p4 * 8);
      OPQ(ta);
      const v8s t0 = cat(trread(ta), trread(ta + 4 * 1088)), t1 = cat(trread(ta + 128), trread(ta + 128 + 4 * 1088));
      st4c<0>(__builtin_bit_cast(v4u, t0), (u32)(l * 16), ws + WS_XNB + tb * 8192 + v * 2048);
      st4c<1024>(__builtin_bit_cast(v4u, t1), (u32)(l * 16), ws + WS_XNB + tb * 8192 + v * 2048);
    }
    SB0();
    const int su = (xcd >> 1) * 4 + (bi & 3), cf0 = (xcd & 1) * 896 + (su < 14 ? su : 0) * 64;
#define A_H1(sb) do { g1_read(s1, s1.xb, s1.wb, sb, 1); g1_mma(s1, s1.xa, s1.wa); g1_sched(); SB0(); } while (0)
#define A_H2(sn) do { g1_read(s1, s1.xa, s1.wa, sn, 0); g1_mma(s1, s1.xb, s1.wb); g1_sched(); SB0(); } while (0)
    if (__builtin_expect(!hasg1, 0)) {
      v4u cr[16];
      const char* cp = (const char*)w1 + (128 * v) * 3584 + cf0 * 2;
#pragma unroll
      for (int i = 0; i < 16; ++i) ld4<0>(cr[i], (u32)((l >> 3) * 3584 + (l & 7) * 16), cp + i * 8 * 3584);
      bar();
      waitv<0>();
      bar();
      if (tid == 0) st1c<0>(tag, 0u, ws + WS_FA + tb * 4);
      float a[8];
#pragma unroll
      for (int j = 0; j < 8; ++j) a[j] = 0.f;
#pragma unroll
      for (int i = 0; i < 16; ++i) {
        PIN(cr[i]);
#pragma unroll
        for (int j = 0; j < 4; ++j) { a[2 * j] += bf2f(cr[i][j]); a[2 * j + 1] += bfhi(cr[i][j]); }
        if (i == 6 || i == 13) { SB0(); bar(); }
      }
#pragma unroll
      for (int j = 0; j < 8; ++j) a[j] = sum32x(sum16x(a[j] + dpp<0x128>(a[j])));
      if (l < 8) {
        *LP(v4f, L_CSP + v * 256 + l * 32) = (v4f){a[0], a[1], a[2], a[3]};
        *LP(v4f, L_CSP + v * 256 + l * 32 + 16) = (v4f){a[4], a[5], a[6], a[7]};
      }
    }
    {
      u32 sb = 0;
      const int nit = hasg1 ? 4 : 0;
#pragma nounroll
      for (int c = 0; c < nit; ++c) {
        A_H1(sb);
        if (c == 1) waitv<0>();
        bar();
        if (c == 1 && tid == 0) st1c<0>(tag, 0u, ws + WS_FA + tb * 4);
        sb = sb == 2 * SLOT ? 0u : sb + SLOT;
        A_H2(sb);
      }
    }
    v2u stg0, stg1, csg0, csg1; v4u pollA, uv0, uv1;
    const char* stp = ws + WS_STAT + (mt1 * 128 + 64 * ti) * 8;
    const char* csp = ws + WS_CS + (nt1 * 128 + 64 * fi) * 8;
    A_H1((u32)SLOT);
    BETA(5);
    if (__builtin_expect(!hasg1 && w == 0, 0)) {
      const float cs = (*LP(float, L_CSP + l * 4) + *LP(float, L_CSP + 256 + l * 4)) + (*LP(float, L_CSP + 512 + l * 4) + *LP(float, L_CSP + 768 + l * 4));
      st2c<0>((v2u){f2u(cs), tag}, (u32)(l * 8), ws + WS_CS + cf0 * 8);
    }
    ld4c<0>(pollA, (u32)(l * 16), ws + WS_FA);
    A_H2((u32)(2 * SLOT)); A_H1((u32)(2 * SLOT));
    BETA(6);
    waitv<0>(); PIN(pollA);
    if (!wall(pollA[0] == tag && pollA[1] == tag && pollA[2] == tag && pollA[3] == tag)) {
      for (u32 spins = 0; spins < kSpin; ++spins) {
        __builtin_amdgcn_s_sleep(4);
        ld4c<0>(pollA, (u32)(l * 16), ws + WS_FA);
        waitv<0>(); PIN(pollA);
        if (wall(pollA[0] == tag && pollA[1] == tag && pollA[2] == tag && pollA[3] == tag)) break;
      }
    }
    if (b == 0 && tid == 0) st1c<0>(tag, 0u, ws + WS_E);
    ld4c<0>(uv0, (u32)((tid >> 1) * 8192 + (tid & 1) * 16), ws + WS_XNB + tb * 32);
    ld4c<0>(uv1, (u32)((tid >> 1) * 8192 + (tid & 1) * 16), ws + WS_XNB + tb * 32 + 128 * 8192);
    A_H2(0u);
    stage2(0, 2 * SLOT);
    A_H1(0u);
    BETA(7);
    ld2c<0>(stg0, (u32)(r32 * 8), stp); ld2c<256>(stg1, (u32)(r32 * 8), stp);
    ld2c<0>(csg0, (u32)(r32 * 8), csp); ld2c<256>(csg1, (u32)(r32 * 8), csp);
    A_H2((u32)SLOT);
    stage2(1, 0);
    waitv<12>(); PIN(uv0); PIN(uv1);
    {
      u32 ua = (u32)(L_U + (tid & 1) * 4608 + 512 + (tid >> 1) * 16);
      OPQ(ua);
      *LP(v4u, ua) = uv0; *LP(v4u, ua + 2048) = uv1;
    }
    A_H1((u32)SLOT);
    BETA(8);
    g1_mma(s1, s1.xb, s1.wb);
    SB0();
    stage2(2, SLOT);
    waitv<8>(); PIN(stg0); PIN(stg1); PIN(csg0); PIN(csg1);
    if (!wall(stg0[1] == tag && stg1[1] == tag && csg0[1] == tag && csg1[1] == tag)) {
      for (u32 spins = 0; spins < kSpin; ++spins) {
        __builtin_amdgcn_s_sleep(4);
        ld2c<0>(stg0, (u32)(r32 * 8), stp); ld2c<256>(stg1, (u32)(r32 * 8), stp);
        ld2c<0>(csg0, (u32)(r32 * 8), csp); ld2c<256>(csg1, (u32)(r32 * 8), csp);
        waitv<0>(); PIN(stg0); PIN(stg1); PIN(csg0); PIN(csg1);
        if (wall(stg0[1] == tag && stg1[1] == tag && csg0[1] == tag && csg1[1] == tag)) break;
      }
    }
    STAMP(21);
    w2read(0, 2 * SLOT);
    {
      v8s ca[2], mb[2];
#pragma unroll
      for (int i = 0; i < 2; ++i) {
        const float cs = u2f(i ? csg1[0] : csg0[0]);
        const float c0 = rbf(cs), c1 = rbf(cs - c0), c2v = (cs - c0) - c1;
        ca[i] = __builtin_bit_cast(v8s, (v4u){(f2u(c0) >> 16) | f2u(c1), pkbf(c2v, 0.f), 0u, 0u});
        const u32 nm = h ? 0u : ((i ? stg1[0] : stg0[0]) & 0xffffu) ^ 0x8000u;
        mb[i] = __builtin_bit_cast(v8s, (v4u){nm | (nm << 16), nm, 0u, 0u});
      }
      s1.acc[0][0] = mfma32(ca[0], mb[0], s1.acc[0][0]); s1.acc[0][1] = mfma32(ca[1], mb[0], s1.acc[0][1]);
      s1.acc[1][0] = mfma32(ca[0], mb[1], s1.acc[1][0]); s1.acc[1][1] = mfma32(ca[1], mb[1], s1.acc[1][1]);
    }
    const rsrc_t rsw = mkrs(ws, nrec);
    const u32 hbase = (u32)(WS_H + ((mt1 * 8 + 4 * ti) * 56 + 4 * nt1 + 2 * fi) * 1024);
    const u32 hoff = (u32)((g & 1) * 56 * 1024 + (h * 16 + c16) * 16);
    const u32 fbase = (u32)(WS_FH + ((mt1 * 4 + 2 * ti) * 32 + 2 * nt1 + fi) * 128);
    auto epi = [&](int tbk, int fb, float den, float ka) __attribute__((always_inline)) {
      {
        const v16f a = s1.acc[tbk][fb];
        u32 hp[8];
#pragma unroll
        for (int j = 0; j < 8; ++j) {
          const float t0 = a[2 * j], t1 = a[2 * j + 1];
          const float d0 = __builtin_amdgcn_exp2f(t0 * ka) * den + den, d1 = __builtin_amdgcn_exp2f(t1 * ka) * den + den;
          hp[j] = pkbf(t0 * __builtin_amdgcn_rcpf(d0), t1 * __builtin_amdgcn_rcpf(d1));
        }
        bst4c<0>((v4u){hp[0], hp[1], hp[2], hp[3]}, hoff, rsw, hbase + (tbk * 112 + fb) * 1024);
        bst4c<512>((v4u){hp[4], hp[5], hp[6], hp[7]}, hoff, rsw, hbase + (tbk * 112 + fb) * 1024);
      }
    };
    const float den0 = bfhi(stg0[0]), den1 = bfhi(stg1[0]);
    const float ka0 = -1.44269504089f * __builtin_amdgcn_rcpf(den0), ka1 = -1.44269504089f * __builtin_amdgcn_rcpf(den1);
    ESTAMP(3);
    waitl(); stage2(3, 2 * SLOT);
    ESTAMP(4);
    epi(0, 0, den0, ka0);
    ESTAMP(5);
    waitv<10>();
    ESTAMP(6);
    w2read(1, 0);
    epi(0, 1, den0, ka0);
    ESTAMP(7);
    waitl(); stage2(4, 0);
    ESTAMP(8);
    waitv<12>();
    ESTAMP(9);
    w2read(2, SLOT);
    epi(1, 0, den1, ka1);
    ESTAMP(10);
    waitv<6>();
    ESTAMP(11);
    if (l == 0) bst1c<0>(tag, rsw, fbase);
    waitl(); stage2(5, SLOT);
    ESTAMP(12);
    w2read(3, 2 * SLOT);
    epi(1, 1, den1, ka1);
    STAMP(22);
    waitl(); stage2(6, 2 * SLOT);
    ESTAMP(13);
    waitv<4>();
    ESTAMP(14);
    if (l == 0) bst1c<0>(tag, rsw, fbase + 4096);
    w2read(4, 0); w2read(5, SLOT);
    waitv<1>();
    w2read(6, 2 * SLOT);
    STAMP(23);
  } else {
    u32 vx[4], vw[4];
#pragma unroll
    for (int i = 0; i < 4; ++i) {
      vx[i] = (u32)((32 * v + (l >> 3)) * 1024 + (((l & 7) ^ ((4 * (i & 1) + g) & 7)) * 16) + i * 7168);
      vw[i] = (u32)((16 * v + g) * 3584 + (((q4 ^ g) * 4 + p4) * 16) + i * (4 * 3584 - 1024));
    }
    const rsrc_t rsX = mkrs((const char*)x + mt1 * 128 * 1024, nrec), rsW = mkrs((const char*)w1 + 256 * nt1, nrec);
    auto dma = [&](int c, int i) __attribute__((always_inline)) {
      const u32 base = (u32)((c % 3) * SLOT + v * 4096);
      SB0();
      if (i == 0) glds1<0>(rsX, c * 128, vx[0], base);
      if (i == 1) glds1<0>(rsW, c * 64 * 3584, vw[0], base + 16384);
      if (i == 2) glds1<1024>(rsX, c * 128, vx[1], base);
      if (i == 3) glds1<1024>(rsW, c * 64 * 3584, vw[1], base + 16384);
      if (i == 4) glds1<2048>(rsX, c * 128, vx[2], base);
      if (i == 5) glds1<2048>(rsW, c * 64 * 3584, vw[2], base + 16384);
      if (i == 6) glds1<3072>(rsX, c * 128, vx[3], base);
      if (i == 7) glds1<3072>(rsW, c * 64 * 3584, vw[3], base + 16384);
      SB0();
    };
    u32 pA, pI, pR, pC, pR2, pC2, pdt;
    const int kch = v >> 1;
    {
      const u32 po = (u32)((tb * 2 * NS + v * 64 + l) * 2);
      ldus(pA, po, logA); ldus(pI, po, Aim); ldus(pR, po, Cre); ldus(pC, po, Cim);
      ldus(pR2, po ^ 128u, Cre); ldus(pC2, po ^ 128u, Cim); ldus(pdt, (u32)((tb * 2 + kch) * 2), logdt);
      ld1c<0>(ev, 0u, ws);
    }
#pragma unroll
    for (int i = 0; i < 8; ++i) dma(0, i);
    init2();
    u32 ta = (u32)(L_TA + (v * 64 + l) * 128 + (((l & 1) | ((l >> 1) & 2) | ((l << 1) & 4)) * 16));
    u32 tbb = (u32)(L_TB + (v * 64 + l) * TB_P);
    PIN(ta); PIN(tbb);
    dma(1, 0); dma(1, 1);
    waitv<10>();
    PIN(ev); PIN(pA); PIN(pI); PIN(pR); PIN(pC); PIN(pR2); PIN(pC2); PIN(pdt);
    STAMP(1);
    tag = (u32)__builtin_amdgcn_readfirstlane(ev) + 1u;
    const float dt = rbf(__builtin_amdgcn_exp2f(bf2f(pdt) * 1.44269504089f));
    const float ar = -rbf(__builtin_amdgcn_exp2f(bf2f(pA) * 1.44269504089f)) * dt * 1.44269504089f;
    const float ai = bf2f(pI) * dt * 0.15915494309f;
    u32 ex = wmaxu(umax(umax(pR & 0x7f80u, pC & 0x7f80u), umax(pR2 & 0x7f80u, pC2 & 0x7f80u))) >> 7;
    dma(1, 2);
    ex = ex < 8u ? 8u : (ex > 250u ? 250u : ex);
    const float sc = u2f((260u - ex) << 23);
    if (l == 0) *LP(float, L_US + kch * 4) = u2f((ex - 6u) << 23) * 2.0f * dt;
    const cf C = {bf2f(pR) * sc, bf2f(pC) * sc};
    const cf z = cexp(ar, ai, 1.f);
    dma(1, 3);
    const cf W = cexp(ar, ai, 128.f), z8 = cexp(ar, -ai, 8.f);
    dma(1, 4);
    auto pack = [&](const cf* p, v4u& re, v4u& im) __attribute__((always_inline)) {
#pragma unroll
      for (int j = 0; j < 4; ++j) { re[j] = pkh(p[2 * j].r, p[2 * j + 1].r); im[j] = pkh(p[2 * j].i, p[2 * j + 1].i); }
    };
    auto putA = [&](int chunk, const cf* p) __attribute__((always_inline)) {
      v4u re, im; pack(p, re, im);
      *LP(v4u, ta ^ (u32)(chunk * 16)) = re; *LP(v4u, ta ^ (u32)(chunk * 16 + 64)) = im;
    };
    auto putB = [&](int chunk, const cf* p) __attribute__((always_inline)) {
      v4u re, im; pack(p, re, im);
      *LP(v4u, tbb + chunk * 16) = re; *LP(v4u, tbb + 48 + chunk * 16) = im;
    };
    cf P[8], Q[8], R[8], S[8], U[8], Y[8], Z[8];
    const cf z2 = cmul(z, z);
    P[0] = C; P[1] = cmul(C, z); P[2] = cmul(P[0], z2); P[3] = cmul(P[1], z2);
    dma(1, 5);
    P[4] = cmul(P[2], z2); P[5] = cmul(P[3], z2); P[6] = cmul(P[4], z2); P[7] = cmul(P[5], z2);
    dma(1, 6);
    putA(0, P);
    dma(1, 7);
    waitv<8>();
    BETA(0);
    dma(2, 0); Q[0] = cmul(P[0], W); Q[1] = cmul(P[1], W);
    dma(2, 1); Q[2] = cmul(P[2], W); Q[3] = cmul(P[3], W);
    dma(2, 2); Q[4] = cmul(P[4], W); Q[5] = cmul(P[5], W);
    dma(2, 3); Q[6] = cmul(P[6], W); Q[7] = cmul(P[7], W);
    dma(2, 4); putA(1, Q);
    dma(2, 5); R[0] = cmul(Q[0], W); R[1] = cmul(Q[2], W);
    dma(2, 6); R[2] = cmul(Q[4], W); R[3] = cmul(Q[6], W);
    dma(2, 7); R[4] = cmul(R[0], W); R[5] = cmul(R[1], W);
    SB0(); waitv<8>(); BETA(1);
    dma(3, 0); R[6] = cmul(R[2], W); R[7] = cmul(R[3], W);
    dma(3, 1); putA(2, R);
    dma(3, 2); S[0] = cmul(R[4], W); S[1] = cmul(R[6], W);
    dma(3, 3); S[2] = cmul(S[0], W); S[3] = cmul(S[1], W);
    dma(3, 4); S[4] = cmul(S[2], W); S[5] = cmul(S[3], W);
    dma(3, 5); S[6] = cmul(S[4], W); S[7] = cmul(S[5], W);
    dma(3, 6); putA(3, S);
    dma(3, 7); const cf W2 = cmul(W, W); Y[0] = {1.f, 0.f}; Y[1] = z8; Y[2] = cmul(z8, z8);
    SB0(); waitv<8>(); BETA(2);
    dma(4, 0); U[0] = cmul(S[4], W2); U[1] = cmul(S[6], W2);
    dma(4, 1); U[2] = cmul(U[0], W2); U[3] = cmul(U[1], W2);
    dma(4, 2); U[4] = cmul(U[2], W2); U[5] = cmul(U[3], W2);
    dma(4, 3); U[6] = cmul(U[4], W2); U[7] = cmul(U[5], W2);
    dma(4, 4); putB(0, U);
    dma(4, 5); { const cf last = cmul(U[6], W2); *LP(v4u, tbb + 96) = (v4u){pkh(last.r, 0.f), 0u, pkh(last.i, 0.f), 0u}; }
    dma(4, 6); Y[3] = cmul(Y[1], Y[2]); Y[4] = cmul(Y[2], Y[2]);
    dma(4, 7); Y[5] = cmul(Y[3], Y[2]); Y[6] = cmul(Y[4], Y[2]);
    SB0(); waitv<8>(); BETA(3);
    dma(5, 0); Y[7] = cmul(Y[5], Y[2]); Z[0] = cmul(Y[4], Y[4]);
    dma(5, 1); putB(1, Y);
    dma(5, 2); Z[1] = cmul(Y[1], Z[0]); Z[2] = cmul(Y[2], Z[0]);
    dma(5, 3); Z[3] = cmul(Y[3], Z[0]); Z[4] = cmul(Y[4], Z[0]);
    dma(5, 4); Z[5] = cmul(Y[5], Z[0]); Z[6] = cmul(Y[6], Z[0]);
    dma(5, 5); Z[7] = cmul(Y[7], Z[0]);
    dma(5, 6); putB(2, Z);
    dma(5, 7);
    const int kt = v & 1, ri = q4 & 1;
    u32 a0[2], a2, ay;
#pragma unroll
    for (int t = 0; t < 2; ++t) {
      const int n = 4 * g + 2 * t + (q4 >> 1);
      a0[t] = (u32)(L_TA + (kch * 128 + n) * 128 + (((ri * 4 + 2 * kt + (p4 >> 1)) ^ ((n & 1) | ((n >> 1) & 2) | ((n << 1) & 4))) * 16) + (p4 & 1) * 8);
      PIN(a0[t]);
    }
    {
      const u32 rb = (u32)(L_TB + (kch * 128 + 4 * g + (q4 >> 1)) * TB_P);
      a2 = rb + (u32)(p4 < 2 ? ri * 48 + p4 * 8 : 96 + ri * 8);
      ay = rb + (u32)(ri * 48 + 16 + p4 * 8);
      PIN(a2); PIN(ay);
    }
    SB0(); waitv<8>(); BETA(4);
    u32 ka[11];
    {
      v8s yb[8], xk[8];
      v4f k0 = {0.f, 0.f, 0.f, 0.f}, k1 = k0;
#pragma unroll
      for (int s = 0; s < 8; ++s) {
        yb[s] = cat(trread(ay + s * 16 * TB_P), trread(ay + s * 16 * TB_P + 2 * TB_P));
        xk[s] = cat(trread(a0[0] + s * 2048), trread(a0[1] + s * 2048));
        if (s & 1) dma(6, s >> 1);
      }
#pragma unroll
      for (int s = 0; s < 8; s += 2) {
        k0 = mfma_h(xk[s], yb[s], k0); k1 = mfma_h(xk[s + 1], yb[s + 1], k1);
        dma(6, 4 + (s >> 1));
      }
#pragma unroll
      for (int s = 0; s < 8; ++s) xk[s] = cat(trread(a2 + s * 16 * TB_P), trread(a2 + s * 16 * TB_P + 2 * TB_P));
      k0 += k1;
      const u32 kb = (u32)(L_KD + kch * KD_P + 8);
      const int base = kt == 0 ? 128 * (g >> 1) + 4 * (g & 1) + 8 * c16 : (g < 2 ? 256 + 64 * g + 4 * c16 : 256 + 64 * g + 2 * c16);
      const int st2 = (kt == 1 && g >= 2) ? 32 : 2;
      *LP(u32, kb + base * 2) = pkh(k0[0], k0[1]);
      *LP(u32, kb + (base + st2) * 2) = pkh(k0[2], k0[3]);
      if (l == 0) *LP(v2u, kb - 8) = (v2u){0u, 0u};
      SB0(); waitv<8>(); BETA(5);
      v4f k2 = {0.f, 0.f, 0.f, 0.f}, k3 = k2;
      const int lagtop = 2047 - 8 * (tid & 255);
      const float lf = (float)lagtop;
      const bool big = lagtop >= 160;
#pragma unroll
      for (int e = 0; e < 12; e += 2) {
        dma(7, e >> 1);
        if (e < 8) { k2 = mfma_h(xk[e], yb[e], k2); k3 = mfma_h(xk[e + 1], yb[e + 1], k3); }
        const u32 pk = pkbf(lf - (float)e, lf - (float)(e + 1));
        ka[e] = (u32)(L_KD + 8 + 2 * (big ? (int)(pk & 0xffffu) - 17024 : lagtop - e));
        PIN(ka[e]);
        if (e + 1 < 11) { ka[e + 1] = (u32)(L_KD + 8 + 2 * (big ? (int)(pk >> 16) - 17024 : lagtop - e - 1)); PIN(ka[e + 1]); }
      }
      dma(7, 6);
      k2 += k3;
      const u32 k01 = pkh(k2[0], k2[1]), k23 = pkh(k2[2], k2[3]);
      u32 kc = kb + (u32)((512 + 64 * g + c16) * 2);
      OPQ(kc);
      if (g < 3) {
        *LP(u16, kc) = (u16)k01; *LP(u16, kc + 32) = (u16)(k01 >> 16);
        *LP(u16, kc + 64) = (u16)k23; *LP(u16, kc + 96) = (u16)(k23 >> 16);
      }
      const int i = tid & 255;
      if (i < 16) *LP(v4u, L_R + (i >> 1) * 4160 + 4096 + (i & 1) * 16) = (v4u){0u, 0u, 0u, 0u};
      if (i >= 64 && i < 128) *LP(v4u, L_U + ((i >> 5) & 1) * 4608 + (i & 31) * 16) = (v4u){0u, 0u, 0u, 0u};
      dma(7, 7);
    }
    waitv<8>(); BETA(6);
    {
      u32 ro = (u32)(L_R + (tid & 255) * 16);
      OPQ(ro);
      auto expand = [&](int ch) __attribute__((always_inline)) {
        u32 kv[11];
#pragma unroll
        for (int e = 0; e < 11; ++e) kv[e] = *LP(u16, ka[e] + ch * KD_P);
        stage2(ch, ch ? 0 : 2 * SLOT);
        u32 pr[10];
#pragma unroll
        for (int e = 0; e < 10; ++e) pr[e] = kv[e] | (kv[e + 1] << 16);
#pragma unroll
        for (int sg = 0; sg < 4; ++sg)
          *LP(v4u, ro + (ch * 4 + sg) * 4160) = (v4u){pr[3 - sg], pr[5 - sg], pr[7 - sg], pr[9 - sg]};
      };
      expand(0);
      waitv<4>(); BETA(7);
      expand(1);
    }
    BETA(8);
    stage2(2, SLOT);
    {
      const int qh = v & 1;
      v4f ya[8];
#pragma unroll
      for (int i = 0; i < 8; ++i) ya[i] = (v4f){0.f, 0.f, 0.f, 0.f};
      u32 fb = (u32)(L_R + (kch * 4 + (c16 & 3)) * 4160 + (2028 - 32 * 59 - 128 * qh + 8 * g - 4 * q4) * 2);
      u32 gb = (u32)(L_U + kch * 4608 + 512 + (16 * c16 - 16 + 8 * g - 128 * qh - 96) * 2);
      OPQ(fb); OPQ(gb);
      auto ldF = [&](int qq, int i) __attribute__((always_inline)) { const u32 a = fb + (3 - qq) * 64 + (7 - i) * 512; return cat(*(volatile LAS v4s*)LP(v4s, a), *(volatile LAS v4s*)LP(v4s, a + 8)); };
      auto ldG = [&](int qq, int i) __attribute__((always_inline)) { return *LP(v8s, gb + (3 - qq) * 64 + i * 512); };
      const u32 pw = (u32)(w * 4096 + (g * 20 + c16) * 16);
      v8s Gf[8], Fq[3];
      Fq[0] = ldF(0, 0);
#pragma unroll
      for (int i = 0; i < 8; ++i) Gf[i] = ldG(0, i);
      Fq[1] = ldF(0, 1);
#pragma unroll
      for (int st = 0; st < 32; ++st) {
        const int qq = st >> 3, i = st & 7;
        if (st + 2 < 32) Fq[(st + 2) % 3] = ldF((st + 2) >> 3, (st + 2) & 7);
        SB0();
#pragma unroll
        for (int n = 7; n >= i; --n) { const int nn = st == 0 ? 7 - n : n; ya[nn] = mfma_h(Fq[st % 3], Gf[nn - i], ya[nn]); }
        SB0();
        if (qq < 3) Gf[7 - i] = ldG(qq + 1, 7 - i);
        else if (i >= 1) *LP(v4f, pw + ((i - 1) / 3) * SLOT + ((i - 1) % 3) * 1280) = ya[i - 1];
        if (st == 0) { waitv<8>(); w2read(0, 2 * SLOT); }
        if (st == 1) { waitl(); stage2(3, 2 * SLOT); }
        if (st == 2) { waitv<8>(); w2read(1, 0); }
        if (st == 3) { waitl(); stage2(4, 0); }
        if (st == 8) { waitv<8>(); w2read(2, SLOT); }
        if (st == 9) { waitl(); stage2(5, SLOT); }
        if (st == 10) { waitv<8>(); w2read(3, 2 * SLOT); }
        if (st == 11) { waitl(); stage2(6, 2 * SLOT); }
        if (st == 16) { waitv<8>(); w2read(4, 0); }
        if (st == 24) { waitv<0>(); w2read(5, SLOT); w2read(6, 2 * SLOT); }
      }
      *LP(v4f, pw + 2 * SLOT + 1280) = ya[7];
    }
    STAMP(23);
  }
  v4u hf[4][7];
  const char* hsrc = ws + WS_H + (tt2 * 4 * 56 + 7 * w) * 1024 + 3072;
  auto hload = [&](int ti) __attribute__((always_inline)) {
    const char* p = hsrc + ti * 56 * 1024;
    ld4c<-3072>(hf[ti][0], (u32)(l * 16), p); ld4c<-2048>(hf[ti][1], (u32)(l * 16), p); ld4c<-1024>(hf[ti][2], (u32)(l * 16), p);
    ld4c<0>(hf[ti][3], (u32)(l * 16), p); ld4c<1024>(hf[ti][4], (u32)(l * 16), p); ld4c<2048>(hf[ti][5], (u32)(l * 16), p);
    ld4c<3072>(hf[ti][6], (u32)(l * 16), p);
  };
  const char* fsrc = ws + WS_FH + (tt2 * 64 + ((7 * w) >> 1)) * 128;
  const u32 foff = (u32)((l & 3) * 128 + (l & 4) * 1024);
  u32 pollH, dsp; v2u xv0, xv1;
  ld1c<0>(pollH, foff, fsrc);
  const u32 ooff = (u32)((((w >> 2) * 16 + (w & 3) * 4 + g) * D + c16 * 4) * 2);
  ld2<0>(xv0, ooff, (const char*)x + (tt2 * 64 * D + dt2 * 64) * 2);
  ld2<0>(xv1, ooff, (const char*)x + ((tt2 * 64 + 32) * D + dt2 * 64) * 2);
  ld1c<0>(dsp, 0u, (const char*)dskip + tb * 4);
  bar();
  STAMP(24);
  {
    const u32 pa = (u32)((w / 3) * SLOT + (w % 3) * 1280 + 16384 + ((l & 3) * 20 + (l >> 2)) * 16);
    v4f y0 = *LP(v4f, pa) + *LP(v4f, pa + 4096);
    v4f y1 = *LP(v4f, pa + 8192) + *LP(v4f, pa + 12288);
    const v4h u0 = *LP(v4h, L_U + 512 + tid * 8), u1 = *LP(v4h, L_U + 4608 + 512 + tid * 8);
    const v2f us = *LP(v2f, L_US);
    bar();
    waitv<0>(); PIN(pollH); PIN(xv0); PIN(xv1); PIN(dsp);
    const float ds0 = bf2f(dsp), ds1 = bfhi(dsp);
    u32 o[4];
#pragma unroll
    for (int j = 0; j < 4; ++j) {
      const u32 yb = pkbf(y0[j] * us[0], y1[j] * us[1]);
      const u32 sk = pkbf((float)u0[j] * ds0, (float)u1[j] * ds1);
      o[j] = pkbf(bf2f(yb) + bf2f(sk), bfhi(yb) + bfhi(sk));
    }
    char* sdst = ws + WS_SG + (w * 4 * 256 + tb) * 512;
    const u32 so = (u32)(g * 256 * 512 + c16 * 16);
    st4c<0>((v4u){o[0], tag, o[1], tag}, so, sdst);
    st4c<256>((v4u){o[2], tag, o[3], tag}, so, sdst);
  }
  STAMP(25);
  if (!wall(pollH == tag)) {
    for (u32 spins = 0; spins < kSpin; ++spins) {
      ld1c<0>(pollH, foff, fsrc);
      waitv<0>(); PIN(pollH);
      if (wall(pollH == tag)) break;
      __builtin_amdgcn_s_sleep(4);
    }
  }
  STAMP(26);
  hload(0); hload(1); hload(2);
  STAMP(27);
  {
    const u32 qw = (u32)(w * 4096 + c16 * 256);
    auto g2 = [&](int ti, u32 qb) __attribute__((always_inline)) {
      v4f a2[4];
#pragma unroll
      for (int dj = 0; dj < 4; ++dj) a2[dj] = (v4f){0.f, 0.f, 0.f, 0.f};
#pragma unroll
      for (int j = 0; j < 7; ++j) PIN(hf[ti][j]);
#pragma unroll
      for (int j = 0; j < 7; ++j)
#pragma unroll
        for (int dj = 0; dj < 4; ++dj) a2[dj] = mfma_bf(wf[j][dj], __builtin_bit_cast(v8s, hf[ti][j]), a2[dj]);
#pragma unroll
      for (int dj = 0; dj < 4; ++dj) *LP(v4f, qb + qw + (((dj * 4 + g) ^ (c16 & 7)) * 16)) = a2[dj];
    };
    const int rt = (w & 3) * 4 + g;
    auto red = [&](u32 qb) __attribute__((always_inline)) {
      u32 a = qb + (u32)(rt * 256 + ((c16 ^ (rt & 7)) * 16));
      OPQ(a);
      v4f sum = ((*LP(v4f, a) + *LP(v4f, a + 4096)) + (*LP(v4f, a + 8192) + *LP(v4f, a + 12288))) +
                ((*LP(v4f, a + 16384) + *LP(v4f, a + 20480)) + (*LP(v4f, a + 24576) + *LP(v4f, a + 28672)));
      const u32 f01 = pkbf(sum[0], sum[1]), f23 = pkbf(sum[2], sum[3]);
      return (v2u){pkbf(silu(bf2f(f01)), silu(bfhi(f01))), pkbf(silu(bf2f(f23)), silu(bfhi(f23)))};
    };
    waitv<14>(); g2(0, 0);
    SB0();
    hload(3);
    v4u sg0, sg1;
    const char* ssrc = ws + WS_SG + (tt2 * 256 + dt2 * 32) * 512;
    ld4c<0>(sg0, (u32)(tid * 16), ssrc); ld4c<0>(sg1, (u32)(tid * 16), ssrc + 8192);
    waitv<16>(); g2(1, SLOT);
    bar();
    STAMP(28);
    waitv<9>(); g2(2, 2 * SLOT);
    const v2u ga = red((u32)((w >> 2) * SLOT));
    waitv<2>(); g2(3, L_R);
    waitv<0>(); PIN(sg0); PIN(sg1);
    if (!wall(sg0[1] == tag && sg0[3] == tag && sg1[1] == tag && sg1[3] == tag)) {
      for (u32 spins = 0; spins < kSpin; ++spins) {
        ld4c<0>(sg0, (u32)(tid * 16), ssrc); ld4c<0>(sg1, (u32)(tid * 16), ssrc + 8192);
        waitv<0>(); PIN(sg0); PIN(sg1);
        if (wall(sg0[1] == tag && sg0[3] == tag && sg1[1] == tag && sg1[3] == tag)) break;
        __builtin_amdgcn_s_sleep(4);
      }
    }
    {
      u32 sa = (u32)(L_S + (2 * c16 + (g & 1)) * 136 + (tid >> 5) * 4);
      OPQ(sa);
      *LP(u32, sa) = sg0[0]; *LP(u32, sa + 4480) = sg0[2];
      *LP(u32, sa + 64) = sg1[0]; *LP(u32, sa + 64 + 4480) = sg1[2];
    }
    bar();
    STAMP(29);
    const v2u gb2 = red((u32)((w >> 2) ? L_R : 2 * SLOT));
    const int tl = (w >> 2) * 16 + rt;
    u32 sr = (u32)(L_S + (tl >> 1) * 136 + (tl & 1) * 4480 + c16 * 8);
    OPQ(sr);
    const v2u sa0 = *LP(v2u, sr), sa1 = *LP(v2u, sr + 16 * 136);
    auto fin = [&](v2u gg, v2u ss, v2u xx) __attribute__((always_inline)) {
      v2u o;
#pragma unroll
      for (int j = 0; j < 2; ++j) {
        const u32 p = pkbf(bf2f(ss[j]) * bf2f(gg[j]), bfhi(ss[j]) * bfhi(gg[j]));
        o[j] = pkbf(bf2f(xx[j]) + bf2f(p), bfhi(xx[j]) + bfhi(p));
      }
      return o;
    };
    GAS char* ob = (GAS char*)out + (tt2 * 64 * D + dt2 * 64) * 2;
    *(GAS v2u*)(ob + ooff) = fin(ga, sa0, xv0);
    *(GAS v2u*)(ob + 32 * D * 2 + ooff) = fin(gb2, sa1, xv1);
  }
  STAMP(30);
}

extern "C" void kernel_launch(void* const* d_in, const int* in_sizes, int n_in, void* d_out, int out_size,
                              void* d_ws, size_t ws_size, hipStream_t stream) {
  fused_kernel<<<256, 512, 0, stream>>>((const u16*)d_in[0], (const u16*)d_in[1], (const u16*)d_in[2],
                                        (const u16*)d_in[3], (const u16*)d_in[4], (const u16*)d_in[5],
                                        (const u16*)d_in[6], (const u16*)d_in[8], (const u16*)d_in[9],
                                        (u16*)d_out, (char*)d_ws);
}
```

```cpp
#define PADK 30
#include <hip/hip_runtime.h>
#include <stdint.h>

typedef unsigned short u16;
typedef unsigned int u32;
typedef unsigned long long u64;
typedef u32 v4u __attribute__((ext_vector_type(4)));
typedef u32 v2u __attribute__((ext_vector_type(2)));
typedef float v4f __attribute__((ext_vector_type(4)));
typedef float v2f __attribute__((ext_vector_type(2)));
typedef float v16f __attribute__((ext_vector_type(16)));
typedef short v8s __attribute__((ext_vector_type(8)));
typedef short v4s __attribute__((ext_vector_type(4)));
typedef __bf16 v8b __attribute__((ext_vector_type(8)));
typedef __bf16 v2b __attribute__((ext_vector_type(2)));
typedef _Float16 v8h __attribute__((ext_vector_type(8)));
typedef _Float16 v4h __attribute__((ext_vector_type(4)));
typedef _Float16 v2h __attribute__((ext_vector_type(2)));
#define LAS __attribute__((address_space(3)))
#define GAS __attribute__((address_space(1)))
#define DEVI __device__ __forceinline__
#if defined(__HIP_DEVICE_COMPILE__)
#define NOPK __attribute__((target("no-packed-fp32-ops")))
#else
#define NOPK
#endif

#define STAMPS 0

constexpr int L = 2048, D = 512, NS = 128, F = 1792;
constexpr int WS_E = 0, WS_FA = 1024, WS_STAT = 8192, WS_CS = 40960;
constexpr int WS_XNB = 65536, WS_SG = WS_XNB + (2 << 20), WS_H = WS_SG + (4 << 20), WS_FH = WS_H + L * F * 2;
constexpr int WS_STAMP = 16 << 20;
constexpr int SLOT = 32768, AUX = 3 * SLOT;
constexpr int L_TA = AUX, L_TB = AUX + 32768, TB_P = 112;
constexpr int L_LN = L_TB;
constexpr int L_KD = AUX + 61440, KD_P = 1488, L_US = L_KD + 2976, L_CSP = L_KD + 3072;
constexpr int L_R = AUX, L_U = AUX + 33280, L_S = AUX + 42496;
constexpr unsigned kSpin = 1u << 20;

__shared__ __attribute__((aligned(16))) char smem[163840];
#define LP(T, off) ((LAS T*)((LAS char*)smem + (off)))
#define PIN(x) asm volatile("" : "+v"(x))
#define OPQ(x) asm("" : "+v"(x))
#define SB0() __builtin_amdgcn_sched_barrier(0)

DEVI float u2f(u32 v) { return __builtin_bit_cast(float, v); }
DEVI u32 f2u(float v) { return __builtin_bit_cast(u32, v); }
DEVI bool wall(bool p) { return __builtin_amdgcn_ballot_w64(!p) == 0; }
DEVI float bf2f(u32 v) { return u2f(v << 16); }
DEVI float bfhi(u32 v) { return u2f(v & 0xffff0000u); }
DEVI u32 pkbf(float a, float b) { v2f v = {a, b}; return __builtin_bit_cast(u32, __builtin_convertvector(v, v2b)); }
DEVI u32 pkh(float a, float b) { v2f v = {a, b}; return __builtin_bit_cast(u32, __builtin_convertvector(v, v2h)); }
DEVI float rbf(float a) { return bf2f(pkbf(a, a)); }
DEVI float silu(float v) { return v * __builtin_amdgcn_rcpf(1.0f + __builtin_amdgcn_exp2f(v * -1.44269504089f)); }
DEVI void waitl() { asm volatile("" ::: "memory"); __builtin_amdgcn_s_waitcnt(0xC07F); asm volatile("" ::: "memory"); }
DEVI void bar() { waitl(); asm volatile("s_barrier" ::: "memory"); }
template <int N> DEVI void waitv() { asm volatile("s_waitcnt vmcnt(%0)" ::"n"(N) : "memory"); }
typedef __amdgpu_buffer_rsrc_t rsrc_t;
DEVI rsrc_t mkrs(const void* p, u32 n) { return __builtin_amdgcn_make_buffer_rsrc((void*)p, 0, n, 0x00020000); }
template <int O> DEVI void glds2(rsrc_t rs, u32 so, u32 va, u32 vb, u32 m) {
  asm volatile("s_mov_b32 m0, %4\n\ts_nop 0\n\tbuffer_load_dwordx4 %0, %2, %3 offen offset:%5 lds\n\tbuffer_load_dwordx4 %1, %2, %3 offen offset:%6 lds"
               ::"v"(va), "v"(vb), "s"(rs), "s"(so), "s"(m), "n"(O), "n"(O + 1024) : "memory");
}
template <int O> DEVI void glds1(rsrc_t rs, u32 so, u32 va, u32 m) {
  asm volatile("s_mov_b32 m0, %3\n\ts_nop 0\n\tbuffer_load_dwordx4 %0, %1, %2 offen offset:%4 lds"
               ::"v"(va), "s"(rs), "s"(so), "s"(m), "n"(O) : "memory");
}
template <int O> DEVI void bst4c(v4u d, u32 voff, rsrc_t rs, u32 so) { asm volatile("buffer_store_dwordx4 %0, %1, %2, %3 offen offset:%4 sc1\n\ts_nop 1" ::"v"(d), "v"(voff), "s"(rs), "s"(so), "n"(O) : "memory"); }
template <int O> DEVI void bst1c(u32 d, rsrc_t rs, u32 so) { asm volatile("buffer_store_dword %0, off, %1, %2 offset:%3 sc1" ::"v"(d), "s"(rs), "s"(so), "n"(O) : "memory"); }
template <int O> DEVI void ld4(v4u& d, u32 voff, const void* sb) { asm volatile("global_load_dwordx4 %0, %1, %2 offset:%3" : "=v"(d) : "v"(voff), "s"(sb), "n"(O) : "memory"); }
template <int O> DEVI void ld4c(v4u& d, u32 voff, const void* sb) { asm volatile("global_load_dwordx4 %0, %1, %2 offset:%3 sc1" : "=v"(d) : "v"(voff), "s"(sb), "n"(O) : "memory"); }
template <int O> DEVI void ld2(v2u& d, u32 voff, const void* sb) { asm volatile("global_load_dwordx2 %0, %1, %2 offset:%3" : "=v"(d) : "v"(voff), "s"(sb), "n"(O) : "memory"); }
template <int O> DEVI void ld2c(v2u& d, u32 voff, const void* sb) { asm volatile("global_load_dwordx2 %0, %1, %2 offset:%3 sc1" : "=v"(d) : "v"(voff), "s"(sb), "n"(O) : "memory"); }
template <int O> DEVI void ld1c(u32& d, u32 voff, const void* sb) { asm volatile("global_load_dword %0, %1, %2 offset:%3 sc1" : "=v"(d) : "v"(voff), "s"(sb), "n"(O) : "memory"); }
DEVI void ldus(u32& d, u32 voff, const void* sb) { asm volatile("global_load_ushort %0, %1, %2" : "=v"(d) : "v"(voff), "s"(sb) : "memory"); }
template <int O> DEVI void st4c(v4u d, u32 voff, void* sb) { asm volatile("global_store_dwordx4 %0, %1, %2 offset:%3 sc1\n\ts_nop 1" ::"v"(voff), "v"(d), "s"(sb), "n"(O) : "memory"); }
template <int O> DEVI void st2c(v2u d, u32 voff, void* sb) { asm volatile("global_store_dwordx2 %0, %1, %2 offset:%3 sc1" ::"v"(voff), "v"(d), "s"(sb), "n"(O) : "memory"); }
template <int O> DEVI void st1c(u32 d, u32 voff, void* sb) { asm volatile("global_store_dword %0, %1, %2 offset:%3 sc1" ::"v"(voff), "v"(d), "s"(sb), "n"(O) : "memory"); }

DEVI v4s trread(u32 off) { return __builtin_amdgcn_ds_read_tr16_b64_v4i16(LP(v4s, off)); }
DEVI v8s cat(v4s a, v4s b) { return __builtin_shufflevector(a, b, 0, 1, 2, 3, 4, 5, 6, 7); }
DEVI v4f mfma_bf(v8s a, v8s b, v4f c) {
  return __builtin_amdgcn_mfma_f32_16x16x32_bf16(__builtin_bit_cast(v8b, a), __builtin_bit_cast(v8b, b), c, 0, 0, 0);
}
DEVI v16f mfma32(v8s a, v8s b, v16f c) {
  return __builtin_amdgcn_mfma_f32_32x32x16_bf16(__builtin_bit_cast(v8b, a), __builtin_bit_cast(v8b, b), c, 0, 0, 0);
}
DEVI v4f mfma_h(v8s a, v8s b, v4f c) {
  return __builtin_amdgcn_mfma_f32_16x16x32_f16(__builtin_bit_cast(v8h, a), __builtin_bit_cast(v8h, b), c, 0, 0, 0);
}
template <int C> DEVI float dpp(float v) { return u2f((u32)__builtin_amdgcn_update_dpp(0, (int)f2u(v), C, 0xf, 0xf, true)); }
DEVI float sum16x(float v) {
  auto a = __builtin_amdgcn_permlane16_swap(f2u(v), f2u(v), false, false);
  return u2f(a[0]) + u2f(a[1]);
}
DEVI float sum32x(float v) {
  auto a = __builtin_amdgcn_permlane32_swap(f2u(v), f2u(v), false, false);
  return u2f(a[0]) + u2f(a[1]);
}
DEVI float wsum(float v) {
  v += dpp<0xB1>(v); v += dpp<0x4E>(v); v += dpp<0x141>(v); v += dpp<0x140>(v);
  return sum32x(sum16x(v));
}
template <int C> DEVI u32 dppu(u32 v) { return (u32)__builtin_amdgcn_update_dpp(0, (int)v, C, 0xf, 0xf, true); }
DEVI u32 umax(u32 a, u32 b) { return a > b ? a : b; }
DEVI u32 wmaxu(u32 v) {
  v = umax(v, dppu<0xB1>(v)); v = umax(v, dppu<0x4E>(v)); v = umax(v, dppu<0x141>(v)); v = umax(v, dppu<0x140>(v));
  auto a = __builtin_amdgcn_permlane16_swap(v, v, false, false);
  v = umax(a[0], a[1]);
  auto b = __builtin_amdgcn_permlane32_swap(v, v, false, false);
  return umax(b[0], b[1]);
}
struct cf { float r, i; };
DEVI cf cmul(cf a, cf b) { return {a.r * b.r - a.i * b.i, a.r * b.i + a.i * b.r}; }
DEVI cf cexp(float ar, float ai, float p) {
  float e = __builtin_amdgcn_exp2f(ar * p);
  float th = __builtin_amdgcn_fractf(ai * p);
  return {e * __builtin_amdgcn_cosf(th), e * __builtin_amdgcn_sinf(th)};
}
#define STAMP(i) do { if ((i) == PADK && b == 0) { while (__builtin_amdgcn_s_memrealtime() - tstart < 4000) __builtin_amdgcn_s_sleep(2); } } while (0)
#define ESTAMP(i)
#define BETA(i) do { bar(); STAMP(2 * (i) + 4); } while (0)

struct G1 {
  v16f acc[2][2];
  v8s xa[2][2], wa[2][2], xb[2][2], wb[2][2];
  u32 xo[4], wo[2];
};
DEVI void g1_read(G1& s, v8s (&xf)[2][2], v8s (&wf)[2][2], u32 sb, int half) {
#pragma unroll
  for (int k = 0; k < 2; ++k) {
    const int ks = half * 2 + k;
    const u32 xa = sb + s.xo[ks];
    xf[k][0] = *LP(v8s, xa);
    wf[k][0] = cat(trread(sb + s.wo[0] + ks * 4096), trread(sb + s.wo[0] + ks * 4096 + 1024));
    wf[k][1] = cat(trread(sb + s.wo[1] + ks * 4096), trread(sb + s.wo[1] + ks * 4096 + 1024));
    xf[k][1] = *LP(v8s, xa + 4096);
  }
}
DEVI void g1_mma(G1& s, v8s (&xf)[2][2], v8s (&wf)[2][2]) {
#pragma unroll
  for (int k = 0; k < 2; ++k) {
    s.acc[0][0] = mfma32(wf[k][0], xf[k][0], s.acc[0][0]);
    s.acc[0][1] = mfma32(wf[k][1], xf[k][0], s.acc[0][1]);
    s.acc[1][0] = mfma32(wf[k][0], xf[k][1], s.acc[1][0]);
    s.acc[1][1] = mfma32(wf[k][1], xf[k][1], s.acc[1][1]);
  }
}
DEVI void g1_sched() {
  __builtin_amdgcn_sched_group_barrier(0x8, 1, 0);
  __builtin_amdgcn_sched_group_barrier(0x2, 4, 0);
#pragma unroll
  for (int i = 0; i < 4; ++i) {
    __builtin_amdgcn_sched_group_barrier(0x100, 3, 0);
    __builtin_amdgcn_sched_group_barrier(0x8, 1, 0);
  }
  __builtin_amdgcn_sched_group_barrier(0x8, 3, 0);
}

NOPK __global__ __launch_bounds__(512, 2) void fused_kernel(
    const u16* x, const u16* logA, const u16* Aim, const u16* Cre, const u16* Cim, const u16* logdt,
    const u16* dskip, const u16* w1, const u16* w2, u16* out, char* ws) {
  const u64 tstart = __builtin_amdgcn_s_memrealtime();
  asm volatile("" : "+s"(x), "+s"(logA), "+s"(Aim), "+s"(Cre), "+s"(Cim), "+s"(logdt), "+s"(dskip), "+s"(w1), "+s"(w2), "+s"(out), "+s"(ws));
  const int tid = __builtin_amdgcn_workitem_id_x(), l = tid & 63, w = __builtin_amdgcn_readfirstlane(tid >> 6);
  const int b = __builtin_amdgcn_workgroup_id_x(), xcd = b & 7, bi = b >> 3;
  const int g = l >> 4, c16 = l & 15, q4 = c16 >> 2, p4 = l & 3, v = w & 3;
  STAMP(0);

  const bool hasg1 = bi < 28;
  const int mt1 = (xcd >> 1) * 4 + (bi & 3), nt1 = (xcd & 1) * 7 + (hasg1 ? bi >> 2 : 0);
  const int tt2 = xcd * 4 + (bi & 3), dt2 = bi >> 2;
  const int tb = xcd * 32 + bi;

  u32 vo2[4], ra[4];
  const rsrc_t rs2 = mkrs((const char*)w2 + (224 * w) * 1024 + dt2 * 128, 224 * 1024);
  const u32 nrec = hasg1 ? 0x7fffffffu : 0u;
  auto init2 = [&]() __attribute__((always_inline)) {
    const int row = 16 * (g >> 1) + 4 * (g & 1) + q4;
#pragma unroll
    for (int i = 0; i < 4; ++i) {
      vo2[i] = (u32)((l >> 3) * 1024 + (((l & 7) ^ ((g & 3) << 1)) * 16) + i * 7168);
      ra[i] = (u32)(w * 4096 + row * 128 + ((i ^ ((row >> 1) & 3)) * 32) + p4 * 8);
    }
  };
  auto stage2 = [&](int j, u32 slotb) __attribute__((always_inline)) {
    glds2<0>(rs2, j * 32768, vo2[0], vo2[1], slotb + (u32)(w * 4096));
    glds2<2048>(rs2, j * 32768, vo2[2], vo2[3], slotb + (u32)(w * 4096));
  };
  v8s wf[7][4];
  auto w2read = [&](int j, u32 slotb) __attribute__((always_inline)) {
#pragma unroll
    for (int dj = 0; dj < 4; ++dj) wf[j][dj] = cat(trread(ra[dj] + slotb), trread(ra[dj] + slotb + 1024));
  };
  u32 ev, tag;

  if (w < 4) {
    v4u xr[2];
    ld4<0>(xr[0], (u32)(l * 16), (const char*)x + (tb * 8 + 2 * v) * 1024);
    ld4<1024>(xr[1], (u32)(l * 16), (const char*)x + (tb * 8 + 2 * v) * 1024);
    ld1c<0>(ev, 0u, ws);
    G1 s1;
    const int ti = v >> 1, fi = v & 1, h = l >> 5, r32 = l & 31;
#pragma unroll
    for (int i = 0; i < 4; ++i) s1.xo[i] = (u32)((64 * ti + r32) * 128 + (((2 * i + h) ^ ((r32 >> 1) & 7)) * 16));
#pragma unroll
    for (int i = 0; i < 2; ++i) s1.wo[i] = (u32)(16384 + (8 * h + q4) * 256 + (((2 * fi + i) ^ q4) * 64) + (g & 1) * 32 + p4 * 8);
#pragma unroll
    for (int i = 0; i < 16; ++i) { s1.acc[0][0][i] = 0.f; s1.acc[0][1][i] = 0.f; s1.acc[1][0][i] = 0.f; s1.acc[1][1][i] = 0.f; }
    PIN(s1.acc[0][0]); PIN(s1.acc[0][1]); PIN(s1.acc[1][0]); PIN(s1.acc[1][1]);
    init2();
    waitv<1>(); PIN(xr[0]); PIN(xr[1]);
    STAMP(1);
    {
      float e[2][8], s[2], q[2], c0[2];
#pragma unroll
      for (int rr = 0; rr < 2; ++rr) {
        c0[rr] = bf2f((u32)__builtin_amdgcn_readfirstlane(xr[rr][0]));
#pragma unroll
        for (int j = 0; j < 4; ++j) { e[rr][2 * j] = bf2f(xr[rr][j]) - c0[rr]; e[rr][2 * j + 1] = bfhi(xr[rr][j]) - c0[rr]; }
        s[rr] = ((e[rr][0] + e[rr][1]) + (e[rr][2] + e[rr][3])) + ((e[rr][4] + e[rr][5]) + (e[rr][6] + e[rr][7]));
        q[rr] = e[rr][0] * e[rr][0];
#pragma unroll
        for (int j = 1; j < 8; ++j) q[rr] += e[rr][j] * e[rr][j];
      }
      auto ps = __builtin_amdgcn_permlane32_swap(f2u(s[0]), f2u(s[1]), false, false);
      auto pq = __builtin_amdgcn_permlane32_swap(f2u(q[0]), f2u(q[1]), false, false);
      auto pm = __builtin_amdgcn_permlane16_swap(f2u(u2f(ps[0]) + u2f(ps[1])), f2u(u2f(pq[0]) + u2f(pq[1])), false, false);
      float rv = u2f(pm[0]) + u2f(pm[1]);
      rv += dpp<0xB1>(rv); rv += dpp<0x4E>(rv); rv += dpp<0x141>(rv); rv += dpp<0x140>(rv);
      rv *= (1.0f / D);
      u32 sg[2];
#pragma unroll
      for (int rr = 0; rr < 2; ++rr) {
        const float ms = u2f((u32)__builtin_amdgcn_readlane((int)f2u(rv), 32 * rr)), mq = u2f((u32)__builtin_amdgcn_readlane((int)f2u(rv), 32 * rr + 16));
        const float var = rbf(__builtin_fmaxf(mq - ms * ms, 0.f));
        const float mb = rbf(ms + c0[rr]);
        const float den = rbf(__builtin_amdgcn_sqrtf(rbf(var + 1e-5f)));
        const float rinv = __builtin_amdgcn_rcpf(den), off = (c0[rr] - mb) * rinv;
        v4u o;
#pragma unroll
        for (int j = 0; j < 4; ++j) o[j] = pkh(e[rr][2 * j] * rinv + off, e[rr][2 * j + 1] * rinv + off);
        *LP(v4u, L_LN + (2 * v + rr) * 1088 + l * 16) = o;
        sg[rr] = (f2u(mb) >> 16) | f2u(den);
      }
      waitv<0>(); PIN(ev);
      tag = (u32)__builtin_amdgcn_readfirstlane(ev) + 1u;
      if (l == 0) st4c<0>((v4u){sg[0], tag, sg[1], tag}, 0u, ws + WS_STAT + (tb * 8 + 2 * v) * 8);
    }
    BETA(0);
    g1_read(s1, s1.xa, s1.wa, 0, 0);
    {
      u32 ta = (u32)(L_LN + q4 * 1088 + v * 256 + g * 32 + p4 * 8);
      OPQ(ta);
      const v8s t0 = cat(trread(ta), trread(ta + 4 * 1088)), t1 = cat(trread(ta + 128), trread(ta + 128 + 4 * 1088));
      st4c<0>(__builtin_bit_cast(v4u, t0), (u32)(l * 16), ws + WS_XNB + tb * 8192 + v * 2048);
      st4c<1024>(__builtin_bit_cast(v4u, t1), (u32)(l * 16), ws + WS_XNB + tb * 8192 + v * 2048);
    }
    SB0();
    const int su = (xcd >> 1) * 4 + (bi & 3), cf0 = (xcd & 1) * 896 + (su < 14 ? su : 0) * 64;
#define A_H1(sb) do { g1_read(s1, s1.xb, s1.wb, sb, 1); g1_mma(s1, s1.xa, s1.wa); g1_sched(); SB0(); } while (0)
#define A_H2(sn) do { g1_read(s1, s1.xa, s1.wa, sn, 0); g1_mma(s1, s1.xb, s1.wb); g1_sched(); SB0(); } while (0)
    if (__builtin_expect(!hasg1, 0)) {
      v4u cr[16];
      const char* cp = (const char*)w1 + (128 * v) * 3584 + cf0 * 2;
#pragma unroll
      for (int i = 0; i < 16; ++i) ld4<0>(cr[i], (u32)((l >> 3) * 3584 + (l & 7) * 16), cp + i * 8 * 3584);
      bar();
      waitv<0>();
      bar();
      if (tid == 0) st1c<0>(tag, 0u, ws + WS_FA + tb * 4);
      float a[8];
#pragma unroll
      for (int j = 0; j < 8; ++j) a[j] = 0.f;
#pragma unroll
      for (int i = 0; i < 16; ++i) {
        PIN(cr[i]);
#pragma unroll
        for (int j = 0; j < 4; ++j) { a[2 * j] += bf2f(cr[i][j]); a[2 * j + 1] += bfhi(cr[i][j]); }
        if (i == 6 || i == 13) { SB0(); bar(); }
      }
#pragma unroll
      for (int j = 0; j < 8; ++j) a[j] = sum32x(sum16x(a[j] + dpp<0x128>(a[j])));
      if (l < 8) {
        *LP(v4f, L_CSP + v * 256 + l * 32) = (v4f){a[0], a[1], a[2], a[3]};
        *LP(v4f, L_CSP + v * 256 + l * 32 + 16) = (v4f){a[4], a[5], a[6], a[7]};
      }
    }
    {
      u32 sb = 0;
      const int nit = hasg1 ? 4 : 0;
#pragma nounroll
      for (int c = 0; c < nit; ++c) {
        A_H1(sb);
        if (c == 1) waitv<0>();
        bar();
        if (c == 1 && tid == 0) st1c<0>(tag, 0u, ws + WS_FA + tb * 4);
        sb = sb == 2 * SLOT ? 0u : sb + SLOT;
        A_H2(sb);
      }
    }
    v2u stg0, stg1, csg0, csg1; v4u pollA, uv0, uv1;
    const char* stp = ws + WS_STAT + (mt1 * 128 + 64 * ti) * 8;
    const char* csp = ws + WS_CS + (nt1 * 128 + 64 * fi) * 8;
    A_H1((u32)SLOT);
    BETA(5);
    if (__builtin_expect(!hasg1 && w == 0, 0)) {
      const float cs = (*LP(float, L_CSP + l * 4) + *LP(float, L_CSP + 256 + l * 4)) + (*LP(float, L_CSP + 512 + l * 4) + *LP(float, L_CSP + 768 + l * 4));
      st2c<0>((v2u){f2u(cs), tag}, (u32)(l * 8), ws + WS_CS + cf0 * 8);
    }
    ld4c<0>(pollA, (u32)(l * 16), ws + WS_FA);
    A_H2((u32)(2 * SLOT)); A_H1((u32)(2 * SLOT));
    BETA(6);
    waitv<0>(); PIN(pollA);
    if (!wall(pollA[0] == tag && pollA[1] == tag && pollA[2] == tag && pollA[3] == tag)) {
      for (u32 spins = 0; spins < kSpin; ++spins) {
        __builtin_amdgcn_s_sleep(4);
        ld4c<0>(pollA, (u32)(l * 16), ws + WS_FA);
        waitv<0>(); PIN(pollA);
        if (wall(pollA[0] == tag && pollA[1] == tag && pollA[2] == tag && pollA[3] == tag)) break;
      }
    }
    if (b == 0 && tid == 0) st1c<0>(tag, 0u, ws + WS_E);
    ld4c<0>(uv0, (u32)((tid >> 1) * 8192 + (tid & 1) * 16), ws + WS_XNB + tb * 32);
    ld4c<0>(uv1, (u32)((tid >> 1) * 8192 + (tid & 1) * 16), ws + WS_XNB + tb * 32 + 128 * 8192);
    A_H2(0u);
    stage2(0, 2 * SLOT);
    A_H1(0u);
    BETA(7);
    ld2c<0>(stg0, (u32)(r32 * 8), stp); ld2c<256>(stg1, (u32)(r32 * 8), stp);
    ld2c<0>(csg0, (u32)(r32 * 8), csp); ld2c<256>(csg1, (u32)(r32 * 8), csp);
    A_H2((u32)SLOT);
    stage2(1, 0);
    waitv<12>(); PIN(uv0); PIN(uv1);
    {
      u32 ua = (u32)(L_U + (tid & 1) * 4608 + 512 + (tid >> 1) * 16);
      OPQ(ua);
      *LP(v4u, ua) = uv0; *LP(v4u, ua + 2048) = uv1;
    }
    A_H1((u32)SLOT);
    BETA(8);
    g1_mma(s1, s1.xb, s1.wb);
    SB0();
    stage2(2, SLOT);
    waitv<8>(); PIN(stg0); PIN(stg1); PIN(csg0); PIN(csg1);
    if (!wall(stg0[1] == tag && stg1[1] == tag && csg0[1] == tag && csg1[1] == tag)) {
      for (u32 spins = 0; spins < kSpin; ++spins) {
        __builtin_amdgcn_s_sleep(4);
        ld2c<0>(stg0, (u32)(r32 * 8), stp); ld2c<256>(stg1, (u32)(r32 * 8), stp);
        ld2c<0>(csg0, (u32)(r32 * 8), csp); ld2c<256>(csg1, (u32)(r32 * 8), csp);
        waitv<0>(); PIN(stg0); PIN(stg1); PIN(csg0); PIN(csg1);
        if (wall(stg0[1] == tag && stg1[1] == tag && csg0[1] == tag && csg1[1] == tag)) break;
      }
    }
    STAMP(21);
    w2read(0, 2 * SLOT);
    {
      v8s ca[2], mb[2];
#pragma unroll
      for (int i = 0; i < 2; ++i) {
        const float cs = u2f(i ? csg1[0] : csg0[0]);
        const float c0 = rbf(cs), c1 = rbf(cs - c0), c2v = (cs - c0) - c1;
        ca[i] = __builtin_bit_cast(v8s, (v4u){(f2u(c0) >> 16) | f2u(c1), pkbf(c2v, 0.f), 0u, 0u});
        const u32 nm = h ? 0u : ((i ? stg1[0] : stg0[0]) & 0xffffu) ^ 0x8000u;
        mb[i] = __builtin_bit_cast(v8s, (v4u){nm | (nm << 16), nm, 0u, 0u});
      }
      s1.acc[0][0] = mfma32(ca[0], mb[0], s1.acc[0][0]); s1.acc[0][1] = mfma32(ca[1], mb[0], s1.acc[0][1]);
      s1.acc[1][0] = mfma32(ca[0], mb[1], s1.acc[1][0]); s1.acc[1][1] = mfma32(ca[1], mb[1], s1.acc[1][1]);
    }
    const rsrc_t rsw = mkrs(ws, nrec);
    const u32 hbase = (u32)(WS_H + ((mt1 * 8 + 4 * ti) * 56 + 4 * nt1 + 2 * fi) * 1024);
    const u32 hoff = (u32)((g & 1) * 56 * 1024 + (h * 16 + c16) * 16);
    const u32 fbase = (u32)(WS_FH + ((mt1 * 4 + 2 * ti) * 32 + 2 * nt1 + fi) * 128);
    auto epi = [&](int tbk, int fb, float den, float ka) __attribute__((always_inline)) {
      {
        const v16f a = s1.acc[tbk][fb];
        u32 hp[8];
#pragma unroll
        for (int j = 0; j < 8; ++j) {
          const float t0 = a[2 * j], t1 = a[2 * j + 1];
          const float d0 = __builtin_amdgcn_exp2f(t0 * ka) * den + den, d1 = __builtin_amdgcn_exp2f(t1 * ka) * den + den;
          hp[j] = pkbf(t0 * __builtin_amdgcn_rcpf(d0), t1 * __builtin_amdgcn_rcpf(d1));
        }
        bst4c<0>((v4u){hp[0], hp[1], hp[2], hp[3]}, hoff, rsw, hbase + (tbk * 112 + fb) * 1024);
        bst4c<512>((v4u){hp[4], hp[5], hp[6], hp[7]}, hoff, rsw, hbase + (tbk * 112 + fb) * 1024);
      }
    };
    const float den0 = bfhi(stg0[0]), den1 = bfhi(stg1[0]);
    const float ka0 = -1.44269504089f * __builtin_amdgcn_rcpf(den0), ka1 = -1.44269504089f * __builtin_amdgcn_rcpf(den1);
    ESTAMP(3);
    waitl(); stage2(3, 2 * SLOT);
    ESTAMP(4);
    epi(0, 0, den0, ka0);
    ESTAMP(5);
    waitv<10>();
    ESTAMP(6);
    w2read(1, 0);
    epi(0, 1, den0, ka0);
    ESTAMP(7);
    waitl(); stage2(4, 0);
    ESTAMP(8);
    waitv<12>();
    ESTAMP(9);
    w2read(2, SLOT);
    epi(1, 0, den1, ka1);
    ESTAMP(10);
    waitv<6>();
    ESTAMP(11);
    if (l == 0) bst1c<0>(tag, rsw, fbase);
    waitl(); stage2(5, SLOT);
    ESTAMP(12);
    w2read(3, 2 * SLOT);
    epi(1, 1, den1, ka1);
    STAMP(22);
    waitl(); stage2(6, 2 * SLOT);
    ESTAMP(13);
    waitv<4>();
    ESTAMP(14);
    if (l == 0) bst1c<0>(tag, rsw, fbase + 4096);
    w2read(4, 0); w2read(5, SLOT);
    waitv<1>();
    w2read(6, 2 * SLOT);
    STAMP(23);
  } else {
    u32 vx[4], vw[4];
#pragma unroll
    for (int i = 0; i < 4; ++i) {
      vx[i] = (u32)((32 * v + (l >> 3)) * 1024 + (((l & 7) ^ ((4 * (i & 1) + g) & 7)) * 16) + i * 7168);
      vw[i] = (u32)((16 * v + g) * 3584 + (((q4 ^ g) * 4 + p4) * 16) + i * (4 * 3584 - 1024));
    }
    const rsrc_t rsX = mkrs((const char*)x + mt1 * 128 * 1024, nrec), rsW = mkrs((const char*)w1 + 256 * nt1, nrec);
    auto dma = [&](int c, int i) __attribute__((always_inline)) {
      const u32 base = (u32)((c % 3) * SLOT + v * 4096);
      SB0();
      if (i == 0) glds1<0>(rsX, c * 128, vx[0], base);
      if (i == 1) glds1<0>(rsW, c * 64 * 3584, vw[0], base + 16384);
      if (i == 2) glds1<1024>(rsX, c * 128, vx[1], base);
      if (i == 3) glds1<1024>(rsW, c * 64 * 3584, vw[1], base + 16384);
      if (i == 4) glds1<2048>(rsX, c * 128, vx[2], base);
      if (i == 5) glds1<2048>(rsW, c * 64 * 3584, vw[2], base + 16384);
      if (i == 6) glds1<3072>(rsX, c * 128, vx[3], base);
      if (i == 7) glds1<3072>(rsW, c * 64 * 3584, vw[3], base + 16384);
      SB0();
    };
    u32 pA, pI, pR, pC, pR2, pC2, pdt;
    const int kch = v >> 1;
    {
      const u32 po = (u32)((tb * 2 * NS + v * 64 + l) * 2);
      ldus(pA, po, logA); ldus(pI, po, Aim); ldus(pR, po, Cre); ldus(pC, po, Cim);
      ldus(pR2, po ^ 128u, Cre); ldus(pC2, po ^ 128u, Cim); ldus(pdt, (u32)((tb * 2 + kch) * 2), logdt);
      ld1c<0>(ev, 0u, ws);
    }
#pragma unroll
    for (int i = 0; i < 8; ++i) dma(0, i);
    init2();
    u32 ta = (u32)(L_TA + (v * 64 + l) * 128 + (((l & 1) | ((l >> 1) & 2) | ((l << 1) & 4)) * 16));
    u32 tbb = (u32)(L_TB + (v * 64 + l) * TB_P);
    PIN(ta); PIN(tbb);
    dma(1, 0); dma(1, 1);
    waitv<10>();
    PIN(ev); PIN(pA); PIN(pI); PIN(pR); PIN(pC); PIN(pR2); PIN(pC2); PIN(pdt);
    STAMP(1);
    tag = (u32)__builtin_amdgcn_readfirstlane(ev) + 1u;
    const float dt = rbf(__builtin_amdgcn_exp2f(bf2f(pdt) * 1.44269504089f));
    const float ar = -rbf(__builtin_amdgcn_exp2f(bf2f(pA) * 1.44269504089f)) * dt * 1.44269504089f;
    const float ai = bf2f(pI) * dt * 0.15915494309f;
    u32 ex = wmaxu(umax(umax(pR & 0x7f80u, pC & 0x7f80u), umax(pR2 & 0x7f80u, pC2 & 0x7f80u))) >> 7;
    dma(1, 2);
    ex = ex < 8u ? 8u : (ex > 250u ? 250u : ex);
    const float sc = u2f((260u - ex) << 23);
    if (l == 0) *LP(float, L_US + kch * 4) = u2f((ex - 6u) << 23) * 2.0f * dt;
    const cf C = {bf2f(pR) * sc, bf2f(pC) * sc};
    const cf z = cexp(ar, ai, 1.f);
    dma(1, 3);
    const cf W = cexp(ar, ai, 128.f), z8 = cexp(ar, -ai, 8.f);
    dma(1, 4);
    auto pack = [&](const cf* p, v4u& re, v4u& im) __attribute__((always_inline)) {
#pragma unroll
      for (int j = 0; j < 4; ++j) { re[j] = pkh(p[2 * j].r, p[2 * j + 1].r); im[j] = pkh(p[2 * j].i, p[2 * j + 1].i); }
    };
    auto putA = [&](int chunk, const cf* p) __attribute__((always_inline)) {
      v4u re, im; pack(p, re, im);
      *LP(v4u, ta ^ (u32)(chunk * 16)) = re; *LP(v4u, ta ^ (u32)(chunk * 16 + 64)) = im;
    };
    auto putB = [&](int chunk, const cf* p) __attribute__((always_inline)) {
      v4u re, im; pack(p, re, im);
      *LP(v4u, tbb + chunk * 16) = re; *LP(v4u, tbb + 48 + chunk * 16) = im;
    };
    cf P[8], Q[8], R[8], S[8], U[8], Y[8], Z[8];
    const cf z2 = cmul(z, z);
    P[0] = C; P[1] = cmul(C, z); P[2] = cmul(P[0], z2); P[3] = cmul(P[1], z2);
    dma(1, 5);
    P[4] = cmul(P[2], z2); P[5] = cmul(P[3], z2); P[6] = cmul(P[4], z2); P[7] = cmul(P[5], z2);
    dma(1, 6);
    putA(0, P);
    dma(1, 7);
    waitv<8>();
    BETA(0);
    dma(2, 0); Q[0] = cmul(P[0], W); Q[1] = cmul(P[1], W);
    dma(2, 1); Q[2] = cmul(P[2], W); Q[3] = cmul(P[3], W);
    dma(2, 2); Q[4] = cmul(P[4], W); Q[5] = cmul(P[5], W);
    dma(2, 3); Q[6] = cmul(P[6], W); Q[7] = cmul(P[7], W);
    dma(2, 4); putA(1, Q);
    dma(2, 5); R[0] = cmul(Q[0], W); R[1] = cmul(Q[2], W);
    dma(2, 6); R[2] = cmul(Q[4], W); R[3] = cmul(Q[6], W);
    dma(2, 7); R[4] = cmul(R[0], W); R[5] = cmul(R[1], W);
    SB0(); waitv<8>(); BETA(1);
    dma(3, 0); R[6] = cmul(R[2], W); R[7] = cmul(R[3], W);
    dma(3, 1); putA(2, R);
    dma(3, 2); S[0] = cmul(R[4], W); S[1] = cmul(R[6], W);
    dma(3, 3); S[2] = cmul(S[0], W); S[3] = cmul(S[1], W);
    dma(3, 4); S[4] = cmul(S[2], W); S[5] = cmul(S[3], W);
    dma(3, 5); S[6] = cmul(S[4], W); S[7] = cmul(S[5], W);
    dma(3, 6); putA(3, S);
    dma(3, 7); const cf W2 = cmul(W, W); Y[0] = {1.f, 0.f}; Y[1] = z8; Y[2] = cmul(z8, z8);
    SB0(); waitv<8>(); BETA(2);
    dma(4, 0); U[0] = cmul(S[4], W2); U[1] = cmul(S[6], W2);
    dma(4, 1); U[2] = cmul(U[0], W2); U[3] = cmul(U[1], W2);
    dma(4, 2); U[4] = cmul(U[2], W2); U[5] = cmul(U[3], W2);
    dma(4, 3); U[6] = cmul(U[4], W2); U[7] = cmul(U[5], W2);
    dma(4, 4); putB(0, U);
    dma(4, 5); { const cf last = cmul(U[6], W2); *LP(v4u, tbb + 96) = (v4u){pkh(last.r, 0.f), 0u, pkh(last.i, 0.f), 0u}; }
    dma(4, 6); Y[3] = cmul(Y[1], Y[2]); Y[4] = cmul(Y[2], Y[2]);
    dma(4, 7); Y[5] = cmul(Y[3], Y[2]); Y[6] = cmul(Y[4], Y[2]);
    SB0(); waitv<8>(); BETA(3);
    dma(5, 0); Y[7] = cmul(Y[5], Y[2]); Z[0] = cmul(Y[4], Y[4]);
    dma(5, 1); putB(1, Y);
    dma(5, 2); Z[1] = cmul(Y[1], Z[0]); Z[2] = cmul(Y[2], Z[0]);
    dma(5, 3); Z[3] = cmul(Y[3], Z[0]); Z[4] = cmul(Y[4], Z[0]);
    dma(5, 4); Z[5] = cmul(Y[5], Z[0]); Z[6] = cmul(Y[6], Z[0]);
    dma(5, 5); Z[7] = cmul(Y[7], Z[0]);
    dma(5, 6); putB(2, Z);
    dma(5, 7);
    const int kt = v & 1, ri = q4 & 1;
    u32 a0[2], a2, ay;
#pragma unroll
    for (int t = 0; t < 2; ++t) {
      const int n = 4 * g + 2 * t + (q4 >> 1);
      a0[t] = (u32)(L_TA + (kch * 128 + n) * 128 + (((ri * 4 + 2 * kt + (p4 >> 1)) ^ ((n & 1) | ((n >> 1) & 2) | ((n << 1) & 4))) * 16) + (p4 & 1) * 8);
      PIN(a0[t]);
    }
    {
      const u32 rb = (u32)(L_TB + (kch * 128 + 4 * g + (q4 >> 1)) * TB_P);
      a2 = rb + (u32)(p4 < 2 ? ri * 48 + p4 * 8 : 96 + ri * 8);
      ay = rb + (u32)(ri * 48 + 16 + p4 * 8);
      PIN(a2); PIN(ay);
    }
    SB0(); waitv<8>(); BETA(4);
    u32 ka[11];
    {
      v8s yb[8], xk[8];
      v4f k0 = {0.f, 0.f, 0.f, 0.f}, k1 = k0;
#pragma unroll
      for (int s = 0; s < 8; ++s) {
        yb[s] = cat(trread(ay + s * 16 * TB_P), trread(ay + s * 16 * TB_P + 2 * TB_P));
        xk[s] = cat(trread(a0[0] + s * 2048), trread(a0[1] + s * 2048));
        if (s & 1) dma(6, s >> 1);
      }
#pragma unroll
      for (int s = 0; s < 8; s += 2) {
        k0 = mfma_h(xk[s], yb[s], k0); k1 = mfma_h(xk[s + 1], yb[s + 1], k1);
        dma(6, 4 + (s >> 1));
      }
#pragma unroll
      for (int s = 0; s < 8; ++s) xk[s] = cat(trread(a2 + s * 16 * TB_P), trread(a2 + s * 16 * TB_P + 2 * TB_P));
      k0 += k1;
      const u32 kb = (u32)(L_KD + kch * KD_P + 8);
      const int base = kt == 0 ? 128 * (g >> 1) + 4 * (g & 1) + 8 * c16 : (g < 2 ? 256 + 64 * g + 4 * c16 : 256 + 64 * g + 2 * c16);
      const int st2 = (kt == 1 && g >= 2) ? 32 : 2;
      *LP(u32, kb + base * 2) = pkh(k0[0], k0[1]);
      *LP(u32, kb + (base + st2) * 2) = pkh(k0[2], k0[3]);
      if (l == 0) *LP(v2u, kb - 8) = (v2u){0u, 0u};
      SB0(); waitv<8>(); BETA(5);
      v4f k2 = {0.f, 0.f, 0.f, 0.f}, k3 = k2;
      const int lagtop = 2047 - 8 * (tid & 255);
      const float lf = (float)lagtop;
      const bool big = lagtop >= 160;
#pragma unroll
      for (int e = 0; e < 12; e += 2) {
        dma(7, e >> 1);
        if (e < 8) { k2 = mfma_h(xk[e], yb[e], k2); k3 = mfma_h(xk[e + 1], yb[e + 1], k3); }
        const u32 pk = pkbf(lf - (float)e, lf - (float)(e + 1));
        ka[e] = (u32)(L_KD + 8 + 2 * (big ? (int)(pk & 0xffffu) - 17024 : lagtop - e));
        PIN(ka[e]);
        if (e + 1 < 11) { ka[e + 1] = (u32)(L_KD + 8 + 2 * (big ? (int)(pk >> 16) - 17024 : lagtop - e - 1)); PIN(ka[e + 1]); }
      }
      dma(7, 6);
      k2 += k3;
      const u32 k01 = pkh(k2[0], k2[1]), k23 = pkh(k2[2], k2[3]);
      u32 kc = kb + (u32)((512 + 64 * g + c16) * 2);
      OPQ(kc);
      if (g < 3) {
        *LP(u16, kc) = (u16)k01; *LP(u16, kc + 32) = (u16)(k01 >> 16);
        *LP(u16, kc + 64) = (u16)k23; *LP(u16, kc + 96) = (u16)(k23 >> 16);
      }
      const int i = tid & 255;
      if (i < 16) *LP(v4u, L_R + (i >> 1) * 4160 + 4096 + (i & 1) * 16) = (v4u){0u, 0u, 0u, 0u};
      if (i >= 64 && i < 128) *LP(v4u, L_U + ((i >> 5) & 1) * 4608 + (i & 31) * 16) = (v4u){0u, 0u, 0u, 0u};
      dma(7, 7);
    }
    waitv<8>(); BETA(6);
    {
      u32 ro = (u32)(L_R + (tid & 255) * 16);
      OPQ(ro);
      auto expand = [&](int ch) __attribute__((always_inline)) {
        u32 kv[11];
#pragma unroll
        for (int e = 0; e < 11; ++e) kv[e] = *LP(u16, ka[e] + ch * KD_P);
        stage2(ch, ch ? 0 : 2 * SLOT);
        u32 pr[10];
#pragma unroll
        for (int e = 0; e < 10; ++e) pr[e] = kv[e] | (kv[e + 1] << 16);
#pragma unroll
        for (int sg = 0; sg < 4; ++sg)
          *LP(v4u, ro + (ch * 4 + sg) * 4160) = (v4u){pr[3 - sg], pr[5 - sg], pr[7 - sg], pr[9 - sg]};
      };
      expand(0);
      waitv<4>(); BETA(7);
      expand(1);
    }
    BETA(8);
    stage2(2, SLOT);
    {
      const int qh = v & 1;
      v4f ya[8];
#pragma unroll
      for (int i = 0; i < 8; ++i) ya[i] = (v4f){0.f, 0.f, 0.f, 0.f};
      u32 fb = (u32)(L_R + (kch * 4 + (c16 & 3)) * 4160 + (2028 - 32 * 59 - 128 * qh + 8 * g - 4 * q4) * 2);
      u32 gb = (u32)(L_U + kch * 4608 + 512 + (16 * c16 - 16 + 8 * g - 128 * qh - 96) * 2);
      OPQ(fb); OPQ(gb);
      auto ldF = [&](int qq, int i) __attribute__((always_inline)) { const u32 a = fb + (3 - qq) * 64 + (7 - i) * 512; return cat(*(volatile LAS v4s*)LP(v4s, a), *(volatile LAS v4s*)LP(v4s, a + 8)); };
      auto ldG = [&](int qq, int i) __attribute__((always_inline)) { return *LP(v8s, gb + (3 - qq) * 64 + i * 512); };
      const u32 pw = (u32)(w * 4096 + (g * 20 + c16) * 16);
      v8s Gf[8], Fq[3];
      Fq[0] = ldF(0, 0);
#pragma unroll
      for (int i = 0; i < 8; ++i) Gf[i] = ldG(0, i);
      Fq[1] = ldF(0, 1);
#pragma unroll
      for (int st = 0; st < 32; ++st) {
        const int qq = st >> 3, i = st & 7;
        if (st + 2 < 32) Fq[(st + 2) % 3] = ldF((st + 2) >> 3, (st + 2) & 7);
        SB0();
#pragma unroll
        for (int n = 7; n >= i; --n) { const int nn = st == 0 ? 7 - n : n; ya[nn] = mfma_h(Fq[st % 3], Gf[nn - i], ya[nn]); }
        SB0();
        if (qq < 3) Gf[7 - i] = ldG(qq + 1, 7 - i);
        else if (i >= 1) *LP(v4f, pw + ((i - 1) / 3) * SLOT + ((i - 1) % 3) * 1280) = ya[i - 1];
        if (st == 0) { waitv<8>(); w2read(0, 2 * SLOT); }
        if (st == 1) { waitl(); stage2(3, 2 * SLOT); }
        if (st == 2) { waitv<8>(); w2read(1, 0); }
        if (st == 3) { waitl(); stage2(4, 0); }
        if (st == 8) { waitv<8>(); w2read(2, SLOT); }
        if (st == 9) { waitl(); stage2(5, SLOT); }
        if (st == 10) { waitv<8>(); w2read(3, 2 * SLOT); }
        if (st == 11) { waitl(); stage2(6, 2 * SLOT); }
        if (st == 16) { waitv<8>(); w2read(4, 0); }
        if (st == 24) { waitv<0>(); w2read(5, SLOT); w2read(6, 2 * SLOT); }
      }
      *LP(v4f, pw + 2 * SLOT + 1280) = ya[7];
    }
    STAMP(23);
  }
  v4u hf[4][7];
  const char* hsrc = ws + WS_H + (tt2 * 4 * 56 + 7 * w) * 1024 + 3072;
  auto hload = [&](int ti) __attribute__((always_inline)) {
    const char* p = hsrc + ti * 56 * 1024;
    ld4c<-3072>(hf[ti][0], (u32)(l * 16), p); ld4c<-2048>(hf[ti][1], (u32)(l * 16), p); ld4c<-1024>(hf[ti][2], (u32)(l * 16), p);
    ld4c<0>(hf[ti][3], (u32)(l * 16), p); ld4c<1024>(hf[ti][4], (u32)(l * 16), p); ld4c<2048>(hf[ti][5], (u32)(l * 16), p);
    ld4c<3072>(hf[ti][6], (u32)(l * 16), p);
  };
  const char* fsrc = ws + WS_FH + (tt2 * 64 + ((7 * w) >> 1)) * 128;
  const u32 foff = (u32)((l & 3) * 128 + (l & 4) * 1024);
  u32 pollH, dsp; v2u xv0, xv1;
  ld1c<0>(pollH, foff, fsrc);
  const u32 ooff = (u32)((((w >> 2) * 16 + (w & 3) * 4 + g) * D + c16 * 4) * 2);
  ld2<0>(xv0, ooff, (const char*)x + (tt2 * 64 * D + dt2 * 64) * 2);
  ld2<0>(xv1, ooff, (const char*)x + ((tt2 * 64 + 32) * D + dt2 * 64) * 2);
  ld1c<0>(dsp, 0u, (const char*)dskip + tb * 4);
  bar();
  STAMP(24);
  {
    const u32 pa = (u32)((w / 3) * SLOT + (w % 3) * 1280 + 16384 + ((l & 3) * 20 + (l >> 2)) * 16);
    v4f y0 = *LP(v4f, pa) + *LP(v4f, pa + 4096);
    v4f y1 = *LP(v4f, pa + 8192) + *LP(v4f, pa + 12288);
    const v4h u0 = *LP(v4h, L_U + 512 + tid * 8), u1 = *LP(v4h, L_U + 4608 + 512 + tid * 8);
    const v2f us = *LP(v2f, L_US);
    bar();
    waitv<0>(); PIN(pollH); PIN(xv0); PIN(xv1); PIN(dsp);
    const float ds0 = bf2f(dsp), ds1 = bfhi(dsp);
    u32 o[4];
#pragma unroll
    for (int j = 0; j < 4; ++j) {
      const u32 yb = pkbf(y0[j] * us[0], y1[j] * us[1]);
      const u32 sk = pkbf((float)u0[j] * ds0, (float)u1[j] * ds1);
      o[j] = pkbf(bf2f(yb) + bf2f(sk), bfhi(yb) + bfhi(sk));
    }
    char* sdst = ws + WS_SG + (w * 4 * 256 + tb) * 512;
    const u32 so = (u32)(g * 256 * 512 + c16 * 16);
    st4c<0>((v4u){o[0], tag, o[1], tag}, so, sdst);
    st4c<256>((v4u){o[2], tag, o[3], tag}, so, sdst);
  }
  STAMP(25);
  if (!wall(pollH == tag)) {
    for (u32 spins = 0; spins < kSpin; ++spins) {
      ld1c<0>(pollH, foff, fsrc);
      waitv<0>(); PIN(pollH);
      if (wall(pollH == tag)) break;
      __builtin_amdgcn_s_sleep(4);
    }
  }
  STAMP(26);
  hload(0); hload(1); hload(2);
  STAMP(27);
  {
    const u32 qw = (u32)(w * 4096 + c16 * 256);
    auto g2 = [&](int ti, u32 qb) __attribute__((always_inline)) {
      v4f a2[4];
#pragma unroll
      for (int dj = 0; dj < 4; ++dj) a2[dj] = (v4f){0.f, 0.f, 0.f, 0.f};
#pragma unroll
      for (int j = 0; j < 7; ++j) PIN(hf[ti][j]);
#pragma unroll
      for (int j = 0; j < 7; ++j)
#pragma unroll
        for (int dj = 0; dj < 4; ++dj) a2[dj] = mfma_bf(wf[j][dj], __builtin_bit_cast(v8s, hf[ti][j]), a2[dj]);
#pragma unroll
      for (int dj = 0; dj < 4; ++dj) *LP(v4f, qb + qw + (((dj * 4 + g) ^ (c16 & 7)) * 16)) = a2[dj];
    };
    const int rt = (w & 3) * 4 + g;
    auto red = [&](u32 qb) __attribute__((always_inline)) {
      u32 a = qb + (u32)(rt * 256 + ((c16 ^ (rt & 7)) * 16));
      OPQ(a);
      v4f sum = ((*LP(v4f, a) + *LP(v4f, a + 4096)) + (*LP(v4f, a + 8192) + *LP(v4f, a + 12288))) +
                ((*LP(v4f, a + 16384) + *LP(v4f, a + 20480)) + (*LP(v4f, a + 24576) + *LP(v4f, a + 28672)));
      const u32 f01 = pkbf(sum[0], sum[1]), f23 = pkbf(sum[2], sum[3]);
      return (v2u){pkbf(silu(bf2f(f01)), silu(bfhi(f01))), pkbf(silu(bf2f(f23)), silu(bfhi(f23)))};
    };
    waitv<14>(); g2(0, 0);
    SB0();
    hload(3);
    v4u sg0, sg1;
    const char* ssrc = ws + WS_SG + (tt2 * 256 + dt2 * 32) * 512;
    ld4c<0>(sg0, (u32)(tid * 16), ssrc); ld4c<0>(sg1, (u32)(tid * 16), ssrc + 8192);
    waitv<16>(); g2(1, SLOT);
    bar();
    STAMP(28);
    waitv<9>(); g2(2, 2 * SLOT);
    const v2u ga = red((u32)((w >> 2) * SLOT));
    waitv<2>(); g2(3, L_R);
    waitv<0>(); PIN(sg0); PIN(sg1);
    if (!wall(sg0[1] == tag && sg0[3] == tag && sg1[1] == tag && sg1[3] == tag)) {
      for (u32 spins = 0; spins < kSpin; ++spins) {
        ld4c<0>(sg0, (u32)(tid * 16), ssrc); ld4c<0>(sg1, (u32)(tid * 16), ssrc + 8192);
        waitv<0>(); PIN(sg0); PIN(sg1);
        if (wall(sg0[1] == tag && sg0[3] == tag && sg1[1] == tag && sg1[3] == tag)) break;
        __builtin_amdgcn_s_sleep(4);
      }
    }
    {
      u32 sa = (u32)(L_S + (2 * c16 + (g & 1)) * 136 + (tid >> 5) * 4);
      OPQ(sa);
      *LP(u32, sa) = sg0[0]; *LP(u32, sa + 4480) = sg0[2];
      *LP(u32, sa + 64) = sg1[0]; *LP(u32, sa + 64 + 4480) = sg1[2];
    }
    bar();
    STAMP(29);
    const v2u gb2 = red((u32)((w >> 2) ? L_R : 2 * SLOT));
    const int tl = (w >> 2) * 16 + rt;
    u32 sr = (u32)(L_S + (tl >> 1) * 136 + (tl & 1) * 4480 + c16 * 8);
    OPQ(sr);
    const v2u sa0 = *LP(v2u, sr), sa1 = *LP(v2u, sr + 16 * 136);
    auto fin = [&](v2u gg, v2u ss, v2u xx) __attribute__((always_inline)) {
      v2u o;
#pragma unroll
      for (int j = 0; j < 2; ++j) {
        const u32 p = pkbf(bf2f(ss[j]) * bf2f(gg[j]), bfhi(ss[j]) * bfhi(gg[j]));
        o[j] = pkbf(bf2f(xx[j]) + bf2f(p), bfhi(xx[j]) + bfhi(p));
      }
      return o;
    };
    GAS char* ob = (GAS char*)out + (tt2 * 64 * D + dt2 * 64) * 2;
    *(GAS v2u*)(ob + ooff) = fin(ga, sa0, xv0);
    *(GAS v2u*)(ob + 32 * D * 2 + ooff) = fin(gb2, sa1, xv1);
  }
  STAMP(30);
}

extern "C" void kernel_launch(void* const* d_in, const int* in_sizes, int n_in, void* d_out, int out_size,
                              void* d_ws, size_t ws_size, hipStream_t stream) {
  fused_kernel<<<256, 512, 0, stream>>>((const u16*)d_in[0], (const u16*)d_in[1], (const u16*)d_in[2],
                                        (const u16*)d_in[3], (const u16*)d_in[4], (const u16*)d_in[5],
                                        (const u16*)d_in[6], (const u16*)d_in[8], (const u16*)d_in[9],
                                        (u16*)d_out, (char*)d_ws);
}
```

```cpp
#include <hip/hip_runtime.h>
#include <stdint.h>

typedef unsigned short u16;
typedef unsigned int u32;
typedef unsigned long long u64;
typedef u32 v4u __attribute__((ext_vector_type(4)));
typedef u32 v2u __attribute__((ext_vector_type(2)));
typedef float v4f __attribute__((ext_vector_type(4)));
typedef float v2f __attribute__((ext_vector_type(2)));
typedef float v16f __attribute__((ext_vector_type(16)));
typedef short v8s __attribute__((ext_vector_type(8)));
typedef short v4s __attribute__((ext_vector_type(4)));
typedef __bf16 v8b __attribute__((ext_vector_type(8)));
typedef __bf16 v2b __attribute__((ext_vector_type(2)));
typedef _Float16 v8h __attribute__((ext_vector_type(8)));
typedef _Float16 v4h __attribute__((ext_vector_type(4)));
typedef _Float16 v2h __attribute__((ext_vector_type(2)));
#define LAS __attribute__((address_space(3)))
#define GAS __attribute__((address_space(1)))
#define DEVI __device__ __forceinline__
#define INL __attribute__((always_inline))

#define PF_CODE 1
#define PF_CODE_BYTES 25600
#define PF_W2 1
#define ROT1 0
#define ROT2 1
#define STAMPS 0

constexpr int L = 2048, D = 512, NS = 128, F = 1792;
constexpr int WS_E = 0, WS_FA = 1024, WS_STAT = 8192, WS_CS = 40960;
constexpr int WS_XNB = 65536, WS_SG = WS_XNB + (2 << 20), WS_H = WS_SG + (4 << 20), WS_FH = WS_H + L * F * 2;
constexpr int WS_STAMP = 16 << 20;
constexpr int SLOT = 32768, AUX = 3 * SLOT;
constexpr int L_TA = AUX, L_TB = AUX + 32768, TB_P = 112;
constexpr int L_LN = L_TB;
constexpr int L_KD = AUX + 61440, KD_P = 1488, L_US = L_KD + 2976, L_CSP = L_KD + 3072;
constexpr int L_R = AUX, L_U = AUX + 33280, L_S = AUX + 42496, L_W3 = AUX + 43008;
constexpr unsigned kSpin = 1u << 20;

__shared__ __attribute__((aligned(16))) char smem[163840];
#define LP(T, off) ((LAS T*)((LAS char*)smem + (off)))
#define PIN(x) asm volatile("" : "+v"(x))
#define OPQ(x) asm("" : "+v"(x))
#define SB0() __builtin_amdgcn_sched_barrier(0)

DEVI float u2f(u32 v) { return __builtin_bit_cast(float, v); }
DEVI u32 f2u(float v) { return __builtin_bit_cast(u32, v); }
DEVI bool wall(bool p) { return __builtin_amdgcn_ballot_w64(!p) == 0; }
DEVI float bf2f(u32 v) { return u2f(v << 16); }
DEVI float bfhi(u32 v) { return u2f(v & 0xffff0000u); }
DEVI u32 pkbf(float a, float b) { v2f v = {a, b}; return __builtin_bit_cast(u32, __builtin_convertvector(v, v2b)); }
DEVI u32 pkh(float a, float b) { v2f v = {a, b}; return __builtin_bit_cast(u32, __builtin_convertvector(v, v2h)); }
DEVI float rbf(float a) { return bf2f(pkbf(a, a)); }
DEVI float silu(float v) { return v * __builtin_amdgcn_rcpf(1.0f + __builtin_amdgcn_exp2f(v * -1.44269504089f)); }
DEVI void waitl() { asm volatile("" ::: "memory"); __builtin_amdgcn_s_waitcnt(0xC07F); asm volatile("" ::: "memory"); }
DEVI void bar() { waitl(); asm volatile("s_barrier" ::: "memory"); }
template <int N> DEVI void waitv() { asm volatile("s_waitcnt vmcnt(%0)" ::"n"(N) : "memory"); }
typedef __amdgpu_buffer_rsrc_t rsrc_t;
DEVI rsrc_t mkrs(const void* p, u32 n) { return __builtin_amdgcn_make_buffer_rsrc((void*)p, 0, n, 0x00020000); }
template <int O> DEVI void glds2(rsrc_t rs, u32 so, u32 va, u32 vb, u32 m) {
  asm volatile("s_mov_b32 m0, %4\n\ts_nop 0\n\tbuffer_load_dwordx4 %0, %2, %3 offen offset:%5 lds\n\tbuffer_load_dwordx4 %1, %2, %3 offen offset:%6 lds"
               ::"v"(va), "v"(vb), "s"(rs), "s"(so), "s"(m), "n"(O), "n"(O + 1024) : "memory");
}
template <int O> DEVI void glds1(rsrc_t rs, u32 so, u32 va, u32 m) {
  asm volatile("s_mov_b32 m0, %3\n\ts_nop 0\n\tbuffer_load_dwordx4 %0, %1, %2 offen offset:%4 lds"
               ::"v"(va), "s"(rs), "s"(so), "s"(m), "n"(O) : "memory");
}
template <int O> DEVI void bst4c(v4u d, u32 voff, rsrc_t rs, u32 so) { asm volatile("buffer_store_dwordx4 %0, %1, %2, %3 offen offset:%4 sc1\n\ts_nop 1" ::"v"(d), "v"(voff), "s"(rs), "s"(so), "n"(O) : "memory"); }
template <int O> DEVI void bst1c(u32 d, rsrc_t rs, u32 so) { asm volatile("buffer_store_dword %0, off, %1, %2 offset:%3 sc1" ::"v"(d), "s"(rs), "s"(so), "n"(O) : "memory"); }
template <int O> DEVI void ld4(v4u& d, u32 voff, const void* sb) { asm volatile("global_load_dwordx4 %0, %1, %2 offset:%3" : "=v"(d) : "v"(voff), "s"(sb), "n"(O) : "memory"); }
template <int O> DEVI void ld4c(v4u& d, u32 voff, const void* sb) { asm volatile("global_load_dwordx4 %0, %1, %2 offset:%3 sc1" : "=v"(d) : "v"(voff), "s"(sb), "n"(O) : "memory"); }
template <int O> DEVI void ld2(v2u& d, u32 voff, const void* sb) { asm volatile("global_load_dwordx2 %0, %1, %2 offset:%3" : "=v"(d) : "v"(voff), "s"(sb), "n"(O) : "memory"); }
template <int O> DEVI void ld2c(v2u& d, u32 voff, const void* sb) { asm volatile("global_load_dwordx2 %0, %1, %2 offset:%3 sc1" : "=v"(d) : "v"(voff), "s"(sb), "n"(O) : "memory"); }
template <int O> DEVI void ld1c(u32& d, u32 voff, const void* sb) { asm volatile("global_load_dword %0, %1, %2 offset:%3 sc1" : "=v"(d) : "v"(voff), "s"(sb), "n"(O) : "memory"); }
DEVI void touch(u32& d, u32 voff, const void* sb) { asm volatile("global_load_dword %0, %1, %2" : "+v"(d) : "v"(voff), "s"(sb) : "memory"); }
DEVI void ldus(u32& d, u32 voff, const void* sb) { asm volatile("global_load_ushort %0, %1, %2" : "=v"(d) : "v"(voff), "s"(sb) : "memory"); }
template <int O> DEVI void st4c(v4u d, u32 voff, void* sb) { asm volatile("global_store_dwordx4 %0, %1, %2 offset:%3 sc1\n\ts_nop 1" ::"v"(voff), "v"(d), "s"(sb), "n"(O) : "memory"); }
template <int O> DEVI void st2c(v2u d, u32 voff, void* sb) { asm volatile("global_store_dwordx2 %0, %1, %2 offset:%3 sc1" ::"v"(voff), "v"(d), "s"(sb), "n"(O) : "memory"); }
template <int O> DEVI void st1c(u32 d, u32 voff, void* sb) { asm volatile("global_store_dword %0, %1, %2 offset:%3 sc1" ::"v"(voff), "v"(d), "s"(sb), "n"(O) : "memory"); }

DEVI v4s trread(u32 off) { return __builtin_amdgcn_ds_read_tr16_b64_v4i16(LP(v4s, off)); }
DEVI v8s cat(v4s a, v4s b) { return __builtin_shufflevector(a, b, 0, 1, 2, 3, 4, 5, 6, 7); }
DEVI v4f mfma_bf(v8s a, v8s b, v4f c) {
  return __builtin_amdgcn_mfma_f32_16x16x32_bf16(__builtin_bit_cast(v8b, a), __builtin_bit_cast(v8b, b), c, 0, 0, 0);
}
DEVI v16f mfma32(v8s a, v8s b, v16f c) {
  return __builtin_amdgcn_mfma_f32_32x32x16_bf16(__builtin_bit_cast(v8b, a), __builtin_bit_cast(v8b, b), c, 0, 0, 0);
}
DEVI v4f mfma_h(v8s a, v8s b, v4f c) {
  return __builtin_amdgcn_mfma_f32_16x16x32_f16(__builtin_bit_cast(v8h, a), __builtin_bit_cast(v8h, b), c, 0, 0, 0);
}
template <int C> DEVI float dpp(float v) { return u2f((u32)__builtin_amdgcn_update_dpp(0, (int)f2u(v), C, 0xf, 0xf, true)); }
DEVI float sum16x(float v) {
  auto a = __builtin_amdgcn_permlane16_swap(f2u(v), f2u(v), false, false);
  return u2f(a[0]) + u2f(a[1]);
}
DEVI float sum32x(float v) {
  auto a = __builtin_amdgcn_permlane32_swap(f2u(v), f2u(v), false, false);
  return u2f(a[0]) + u2f(a[1]);
}
DEVI float wsum(float v) {
  v += dpp<0xB1>(v); v += dpp<0x4E>(v); v += dpp<0x141>(v); v += dpp<0x140>(v);
  return sum32x(sum16x(v));
}
template <int C> DEVI u32 dppu(u32 v) { return (u32)__builtin_amdgcn_update_dpp(0, (int)v, C, 0xf, 0xf, true); }
DEVI u32 umax(u32 a, u32 b) { return a > b ? a : b; }
DEVI u32 wmaxu(u32 v) {
  v = umax(v, dppu<0xB1>(v)); v = umax(v, dppu<0x4E>(v)); v = umax(v, dppu<0x141>(v)); v = umax(v, dppu<0x140>(v));
  auto a = __builtin_amdgcn_permlane16_swap(v, v, false, false);
  v = umax(a[0], a[1]);
  auto b = __builtin_amdgcn_permlane32_swap(v, v, false, false);
  return umax(b[0], b[1]);
}
struct cf { float r, i; };
DEVI cf cmul(cf a, cf b) { return {a.r * b.r - a.i * b.i, a.r * b.i + a.i * b.r}; }
DEVI void cmul2(cf& o0, cf a0, cf& o1, cf a1, cf b) {
  asm("v_mul_f32 %0, %5, %9\n\tv_mul_f32 %1, %4, %9\n\tv_mul_f32 %2, %7, %9\n\tv_mul_f32 %3, %6, %9\n\t"
      "v_fma_f32 %0, %4, %8, -%0\n\tv_fmac_f32 %1, %5, %8\n\tv_fma_f32 %2, %6, %8, -%2\n\tv_fmac_f32 %3, %7, %8"
      : "=&v"(o0.r), "=&v"(o0.i), "=&v"(o1.r), "=&v"(o1.i) : "v"(a0.r), "v"(a0.i), "v"(a1.r), "v"(a1.i), "v"(b.r), "v"(b.i));
}
struct c2 { v2f r, i; };
DEVI c2 mulS(c2 a, cf s) { return {a.r * s.r - a.i * s.i, a.r * s.i + a.i * s.r}; }
DEVI u32 pkh2(v2f v) { return __builtin_bit_cast(u32, __builtin_convertvector(v, v2h)); }
DEVI u32 pkbf2(v2f v) { return __builtin_bit_cast(u32, __builtin_convertvector(v, v2b)); }
DEVI cf cexp(float ar, float ai, float p) {
  float e = __builtin_amdgcn_exp2f(ar * p);
  float th = __builtin_amdgcn_fractf(ai * p);
  return {e * __builtin_amdgcn_cosf(th), e * __builtin_amdgcn_sinf(th)};
}
#define STAMP(i)
#define BETA(i) bar()
#define ESTAMP(i)
#define PADINIT()

struct G1F { v8s x[4][2], w[4][2]; };
struct G1 {
  v16f acc[2][2];
  G1F fa, fb;
  u32 xo[4], wo[2];
};
DEVI float sum4(float a, float b, float c, float d) {
  float r, t;
  asm("v_add_f32 %0, %2, %3\n\tv_add_f32 %1, %4, %5\n\tv_add_f32 %0, %0, %1" : "=&v"(r), "=&v"(t) : "v"(a), "v"(b), "v"(c), "v"(d));
  return r;
}
DEVI float fma1(float a, float b, float c) { float r; asm("v_fma_f32 %0, %1, %2, %3" : "=v"(r) : "v"(a), "v"(b), "v"(c)); return r; }
DEVI float mul1(float a, float b) { float r; asm("v_mul_f32 %0, %1, %2" : "=v"(r) : "v"(a), "v"(b)); return r; }
template <int O> DEVI void gldsn(rsrc_t rs, u32 so, u32 va) {
  asm volatile("buffer_load_dwordx4 %0, %1, %2 offen offset:%3 lds" ::"v"(va), "s"(rs), "s"(so), "n"(O) : "memory");
}
template <bool FIRST, bool MMA, bool READ> DEVI void g1_step(G1& s, G1F& c, G1F& n, u32 sb) {
  const v16f zero = {0.f, 0.f, 0.f, 0.f, 0.f, 0.f, 0.f, 0.f, 0.f, 0.f, 0.f, 0.f, 0.f, 0.f, 0.f, 0.f};
  const u32 w0 = sb + s.wo[0], w1 = sb + s.wo[1];
#pragma unroll
  for (int m = 0; m < 16; ++m) {
    const int k = m >> 2, tb = (m >> 1) & 1, fb = m & 1;
    if (MMA) s.acc[tb][fb] = mfma32(c.w[k][fb], c.x[k][tb], (FIRST && k == 0) ? zero : s.acc[tb][fb]);
    if (READ && m < 8) {
      const int kr = m >> 1;
      if ((m & 1) == 0) {
        n.x[kr][0] = *LP(v8s, sb + s.xo[kr]);
        n.w[kr][0] = cat(trread(w0 + kr * 4096), trread(w0 + kr * 4096 + 1024));
      } else {
        n.w[kr][1] = cat(trread(w1 + kr * 4096), trread(w1 + kr * 4096 + 1024));
        n.x[kr][1] = *LP(v8s, sb + s.xo[kr] + 4096);
      }
      SB0();
    }
  }
  if (READ) SB0();
}

__global__ __launch_bounds__(512, 2) void fused_kernel(
    const u16* x, const u16* logA, const u16* Aim, const u16* Cre, const u16* Cim, const u16* logdt,
    const u16* dskip, const u16* w1, const u16* w2, u16* out, char* ws) {
  PADINIT();
  u32 junk = 0;
  if (__builtin_amdgcn_workgroup_id_x() < 32) {
    const u32 o = __builtin_amdgcn_workitem_id_x() * 128;
    if (o < PF_CODE_BYTES) touch(junk, o, (const char*)(__builtin_amdgcn_s_getpc() & ~255ull));
  }
  asm volatile("" : "+s"(x), "+s"(logA), "+s"(Aim), "+s"(Cre), "+s"(Cim), "+s"(logdt), "+s"(dskip), "+s"(w1), "+s"(w2), "+s"(out), "+s"(ws));
  const int tid = __builtin_amdgcn_workitem_id_x(), l = tid & 63, w = __builtin_amdgcn_readfirstlane(tid >> 6);
  const int b = __builtin_amdgcn_workgroup_id_x(), xcd = b & 7, bi = b >> 3;
  const int g = l >> 4, c16 = l & 15, q4 = c16 >> 2, p4 = l & 3, v = w & 3;
  STAMP(0);

  const bool hasg1 = bi >= 4;
  const int mt1 = (xcd >> 1) * 4 + (bi & 3), nt1 = (xcd & 1) * 7 + (hasg1 ? (bi >> 2) - 1 : 0);
  const int tt2 = xcd * 4 + (bi & 3), dt2 = bi >> 2;
  const int tb = xcd * 32 + bi;
  const u32 nrec = hasg1 ? 0x7fffffffu : 0u;
  const u32 l16 = (u32)(l * 16);

  u32 vo2[4], ra[4];
  rsrc_t rs2, rsX, rsW;
  auto init2 = [&]() INL {
    asm volatile("" : "+s"(w2));
    rs2 = mkrs((const char*)w2 + dt2 * 128, F * D * 2 - dt2 * 128);
    const int row = 16 * (g >> 1) + 4 * (g & 1) + q4;
#pragma unroll
    for (int i = 0; i < 4; ++i) {
      vo2[i] = (u32)((l >> 3) * 1024 + (((l & 7) ^ ((g & 3) << 1)) * 16) + i * 7168);
      ra[i] = (u32)(w * 4096 + row * 128 + ((i ^ ((row >> 1) & 3)) * 32) + p4 * 8);
      PIN(vo2[i]); PIN(ra[i]);
    }
  };
  auto dma2 = [&](int ww, int j, u32 slotb, int i) INL {
    const u32 so = (u32)((224 * ww + 32 * j) * 1024);
    if (i == 0) glds1<0>(rs2, so, vo2[0], slotb + (u32)(ww * 4096));
    if (i == 1) gldsn<1024>(rs2, so, vo2[1]);
    if (i == 2) gldsn<2048>(rs2, so, vo2[2]);
    if (i == 3) gldsn<3072>(rs2, so, vo2[3]);
  };
  auto stage2w = [&](int ww, int j, u32 slotb) INL {
#pragma unroll
    for (int i = 0; i < 4; ++i) dma2(ww, j, slotb, i);
  };
  auto stage2 = [&](int j, u32 slotb) INL { stage2w(w, j, slotb); };
  v8s wf[7][4];
  auto w2read = [&](int j, u32 slotb) INL {
#pragma unroll
    for (int dj = 0; dj < 4; ++dj) wf[j][dj] = cat(trread(ra[dj] + slotb), trread(ra[dj] + slotb + 1024));
  };
  u32 ev, tag;
  u32 vx[4], vw[4];
  auto init1 = [&]() INL {
    const u16 *xx = x, *ww = w1;
    asm volatile("" : "+s"(xx), "+s"(ww));
    rsX = mkrs((const char*)xx + mt1 * 128 * 1024, nrec); rsW = mkrs((const char*)ww + 256 * nt1, nrec);
#pragma unroll
    for (int i = 0; i < 4; ++i) {
      vx[i] = (u32)((32 * v + (l >> 3)) * 1024 + (((l & 7) ^ ((4 * (i & 1) + g) & 7)) * 16) + i * 7168);
      vw[i] = (u32)((16 * v + g) * 3584 + (((q4 ^ g) * 4 + p4) * 16) + i * (4 * 3584 - 1024));
    }
  };
  auto dma = [&](int c, int i) INL {
    const u32 base = (u32)((c % 3) * SLOT + v * 4096);
    const u32 kc = (u32)((c + ROT1 * ((bi >> 2) + 2 * bi)) & 7);
    SB0();
    if (i == 0) glds1<0>(rsX, kc * 128, vx[0], base);
    if (i == 1) gldsn<1024>(rsX, kc * 128, vx[1]);
    if (i == 2) gldsn<2048>(rsX, kc * 128, vx[2]);
    if (i == 3) gldsn<3072>(rsX, kc * 128, vx[3]);
    if (i == 4) glds1<0>(rsW, kc * (64 * 3584), vw[0], base + 16384);
    if (i == 5) gldsn<1024>(rsW, kc * (64 * 3584), vw[1]);
    if (i == 6) gldsn<2048>(rsW, kc * (64 * 3584), vw[2]);
    if (i == 7) gldsn<3072>(rsW, kc * (64 * 3584), vw[3]);
    SB0();
  };

  if (w < 4) {
    v4u xr[2];
    auto lnload = [&]() INL {
      ld4<0>(xr[0], l16, (const char*)x + (tb * 8 + 2 * v) * 1024);
      ld4<1024>(xr[1], l16, (const char*)x + (tb * 8 + 2 * v) * 1024);
      ld1c<0>(ev, 0u, ws);
    };
    auto layernorm = [&](bool withdma) INL {
      tag = (u32)__builtin_amdgcn_readfirstlane(ev) + 1u;
      v2f e[2][4];
      float s[2], q[2], c0[2];
#pragma unroll
      for (int rr = 0; rr < 2; ++rr) {
        c0[rr] = bf2f((u32)__builtin_amdgcn_readfirstlane(xr[rr][0]));
#pragma unroll
        for (int j = 0; j < 4; ++j) e[rr][j] = (v2f){bf2f(xr[rr][j]), bfhi(xr[rr][j])} - c0[rr];
        const v2f s2 = (e[rr][0] + e[rr][1]) + (e[rr][2] + e[rr][3]);
        const v2f q2 = (e[rr][0] * e[rr][0] + e[rr][1] * e[rr][1]) + (e[rr][2] * e[rr][2] + e[rr][3] * e[rr][3]);
        s[rr] = s2[0] + s2[1]; q[rr] = q2[0] + q2[1];
        if (withdma) dma(2, rr);
      }
      auto ps = __builtin_amdgcn_permlane32_swap(f2u(s[0]), f2u(s[1]), false, false);
      auto pq = __builtin_amdgcn_permlane32_swap(f2u(q[0]), f2u(q[1]), false, false);
      auto pm = __builtin_amdgcn_permlane16_swap(f2u(u2f(ps[0]) + u2f(ps[1])), f2u(u2f(pq[0]) + u2f(pq[1])), false, false);
      float rv = u2f(pm[0]) + u2f(pm[1]);
      rv += dpp<0xB1>(rv); rv += dpp<0x4E>(rv); rv += dpp<0x141>(rv); rv += dpp<0x140>(rv);
      rv *= (1.0f / D);
      if (withdma) dma(2, 2);
      u32 sg[2];
      v4u o[2];
#pragma unroll
      for (int rr = 0; rr < 2; ++rr) {
        const float ms = u2f((u32)__builtin_amdgcn_readlane((int)f2u(rv), 32 * rr)), mq = u2f((u32)__builtin_amdgcn_readlane((int)f2u(rv), 32 * rr + 16));
        const float var = rbf(__builtin_fmaxf(mq - ms * ms, 0.f));
        const float mb = rbf(ms + c0[rr]);
        const float den = rbf(__builtin_amdgcn_sqrtf(rbf(var + 1e-5f)));
        const float rinv = __builtin_amdgcn_rcpf(den), off = (c0[rr] - mb) * rinv;
        if (withdma) dma(2, 3 + 2 * rr);
#pragma unroll
        for (int j = 0; j < 4; ++j) o[rr][j] = pkh2(e[rr][j] * rinv + off);
        sg[rr] = (f2u(mb) >> 16) | f2u(den);
        if (withdma) dma(2, 4 + 2 * rr);
      }
      if (withdma) dma(2, 7);
      u32 lna = (u32)(L_LN + 2 * v * 1088) + l16;
      OPQ(lna);
      *LP(v4u, lna) = o[0]; *LP(v4u, lna + 1088) = o[1];
      if (l == 0) st4c<0>((v4u){sg[0], tag, sg[1], tag}, 0u, ws + WS_STAT + (tb * 8 + 2 * v) * 8);
      STAMP(2);
    };
    if (__builtin_expect(!hasg1, 0)) {
      lnload();
      asm volatile("s_barrier" ::: "memory");
      const int su = (xcd >> 1) * 4 + (bi & 3), cf0 = (xcd & 1) * 896 + (su < 14 ? su : 0) * 64;
      v4u cr[16];
      const char* cp = (const char*)w1 + (128 * v) * 3584 + cf0 * 2;
#pragma unroll
      for (int i = 0; i < 16; ++i) ld4<0>(cr[i], (u32)((l >> 3) * 3584 + (l & 7) * 16), cp + i * 8 * 3584);
      waitv<16>(); PIN(junk); PIN(xr[0]); PIN(xr[1]); PIN(ev);
      layernorm(false);
      bar();
      waitv<1>();
      v2f a[4];
#pragma unroll
      for (int j = 0; j < 4; ++j) a[j] = (v2f){0.f, 0.f};
#pragma unroll
      for (int i = 0; i < 16; ++i) {
        PIN(cr[i]);
#pragma unroll
        for (int j = 0; j < 4; ++j) a[j] += (v2f){bf2f(cr[i][j]), bfhi(cr[i][j])};
        if (i == 9) { SB0(); bar(); }
      }
      float r8[8];
#pragma unroll
      for (int j = 0; j < 8; ++j) { const float t = a[j >> 1][j & 1]; r8[j] = sum32x(sum16x(t + dpp<0x128>(t))); }
      if (l < 8) {
        *LP(v4f, L_CSP + v * 256 + l * 32) = (v4f){r8[0], r8[1], r8[2], r8[3]};
        *LP(v4f, L_CSP + v * 256 + l * 32 + 16) = (v4f){r8[4], r8[5], r8[6], r8[7]};
      }
      bar();
      if (w == 0) {
        const float cs = (*LP(float, L_CSP + l * 4) + *LP(float, L_CSP + 256 + l * 4)) + (*LP(float, L_CSP + 512 + l * 4) + *LP(float, L_CSP + 768 + l * 4));
        st2c<0>((v2u){f2u(cs), tag}, (u32)(l * 8), ws + WS_CS + cf0 * 8);
      }
      init2();
#pragma nounroll
      for (int c = 3; c <= 9; ++c) bar();
    } else {
      lnload();
      init1();
      asm volatile("s_barrier" ::: "memory");
#pragma unroll
      for (int i = 0; i < 8; ++i) dma(0, i);
      G1 s1;
      const int ti = v >> 1, fi = v & 1, h = l >> 5, r32 = l & 31;
#pragma unroll
      for (int i = 0; i < 4; ++i) { s1.xo[i] = (u32)((64 * ti + r32) * 128 + (((2 * i + h) ^ ((r32 >> 1) & 7)) * 16)); PIN(s1.xo[i]); }
#pragma unroll
      for (int i = 0; i < 2; ++i) { s1.wo[i] = (u32)(16384 + (8 * h + q4) * 256 + (((2 * fi + i) ^ q4) * 64) + (g & 1) * 32 + p4 * 8); PIN(s1.wo[i]); }
      u32 hoff = (u32)((g & 1) * 56 * 1024 + (h * 16 + c16) * 16);
      PIN(hoff);
      waitv<8>(); PIN(xr[0]); PIN(xr[1]); PIN(ev);
      STAMP(1);
      layernorm(true);
      waitv<9>();
      BETA(0);
      g1_step<false, false, true>(s1, s1.fb, s1.fa, 0u);
      init2();
      BETA(1);
      g1_step<true, true, true>(s1, s1.fa, s1.fb, (u32)SLOT);
      waitv<0>();
      BETA(2);
      {
        u32 sb = 2 * SLOT;
#pragma nounroll
        for (int c = 2; c < 8; c += 2) {
          g1_step<false, true, true>(s1, s1.fb, s1.fa, sb);
          BETA(c + 1);
          sb = sb == 2 * SLOT ? 0u : sb + SLOT;
          g1_step<false, true, true>(s1, s1.fa, s1.fb, sb);
          BETA(c + 2);
          sb = sb == 2 * SLOT ? 0u : sb + SLOT;
        }
      }
      __builtin_amdgcn_s_setprio(2);
      u32 sta = (u32)(L_CSP + (64 * ti + r32) * 4), csa = (u32)(L_CSP + 512 + (64 * fi + r32) * 4);
      OPQ(sta); OPQ(csa);
      const u32 stg0 = *LP(u32, sta), stg1 = *LP(u32, sta + 128), csg0 = *LP(u32, csa), csg1 = *LP(u32, csa + 128);
      g1_step<false, true, false>(s1, s1.fb, s1.fa, 0u);
      float den0 = bfhi(stg0), den1 = bfhi(stg1), ka0, ka1;
      {
        v8s ca[2], mb[2];
#pragma unroll
        for (int i = 0; i < 2; ++i) {
          float cs = u2f(i ? csg1 : csg0);
          const float c0 = rbf(cs);
          cs -= c0; OPQ(cs);
          const float c1 = rbf(cs);
          cs -= c1; OPQ(cs);
          ca[i] = __builtin_bit_cast(v8s, (v4u){(f2u(c0) >> 16) | f2u(c1), pkbf(cs, 0.f), 0u, 0u});
          const u32 nm = h ? 0u : ((i ? stg1 : stg0) & 0xffffu) ^ 0x8000u;
          mb[i] = __builtin_bit_cast(v8s, (v4u){nm | (nm << 16), nm, 0u, 0u});
        }
        ka0 = -1.44269504089f * __builtin_amdgcn_rcpf(den0); OPQ(ka0);
        ka1 = -1.44269504089f * __builtin_amdgcn_rcpf(den1); OPQ(ka1);
        s1.acc[0][0] = mfma32(ca[0], mb[0], s1.acc[0][0]); s1.acc[0][1] = mfma32(ca[1], mb[0], s1.acc[0][1]);
        s1.acc[1][0] = mfma32(ca[0], mb[1], s1.acc[1][0]); s1.acc[1][1] = mfma32(ca[1], mb[1], s1.acc[1][1]);
        __builtin_amdgcn_sched_group_barrier(0x2, 4, 0);
        __builtin_amdgcn_sched_group_barrier(0x100, 4, 0);
        __builtin_amdgcn_sched_group_barrier(0x8, 4, 0);
#pragma unroll
        for (int i = 0; i < 12; ++i) { __builtin_amdgcn_sched_group_barrier(0x8, 1, 0); __builtin_amdgcn_sched_group_barrier(0x2, 4, 0); }
        __builtin_amdgcn_sched_group_barrier(0x8, 4, 0);
        SB0();
      }
      STAMP(21);
      char* hbase = ws + WS_H + ((mt1 * 8 + 4 * ti) * 56 + 4 * nt1 + 2 * fi) * 1024;
      auto epi = [&](int tbk, int fb, float den, float ka) INL {
        const v16f a = s1.acc[tbk][fb];
        u32 hp[8];
#pragma unroll
        for (int j = 0; j < 8; ++j) {
          const v2f t = {a[2 * j], a[2 * j + 1]};
          const v2f ar = t * ka;
          const v2f d = (v2f){__builtin_amdgcn_exp2f(ar[0]), __builtin_amdgcn_exp2f(ar[1])} * den + den;
          hp[j] = pkbf2(t * (v2f){__builtin_amdgcn_rcpf(d[0]), __builtin_amdgcn_rcpf(d[1])});
          if (j == 3) st4c<0>((v4u){hp[0], hp[1], hp[2], hp[3]}, hoff, hbase + (tbk * 112 + fb) * 1024);
        }
        st4c<512>((v4u){hp[4], hp[5], hp[6], hp[7]}, hoff, hbase + (tbk * 112 + fb) * 1024);
      };
      epi(0, 0, den0, ka0);
      ESTAMP(3);
      asm volatile("s_barrier" ::: "memory");
      epi(0, 1, den0, ka0);
      ESTAMP(4);
      epi(1, 0, den1, ka1);
      ESTAMP(5);
      epi(1, 1, den1, ka1);
      ESTAMP(6);
      waitv<0>();
      if (l == 0) st1c<0>(tag, 0u, ws + WS_FH + ((mt1 * 2 + ti) * 32 + 2 * nt1 + fi) * 128);
      __builtin_amdgcn_s_setprio(0);
    }
    STAMP(22);
    bar();
    w2read(0, 2 * SLOT); w2read(1, 0); w2read(2, SLOT); w2read(3, L_W3);
    STAMP(23);
  } else {
    __builtin_amdgcn_s_setprio(1);
    u32 pA, pI, pR, pC, pR2, pC2, pdt;
    const int kch = v >> 1;
    {
      const u32 po = (u32)((tb * 2 * NS + v * 64 + l) * 2);
      ldus(pA, po, logA); ldus(pI, po, Aim); ldus(pR, po, Cre); ldus(pC, po, Cim);
      ldus(pR2, po ^ 128u, Cre); ldus(pC2, po ^ 128u, Cim); ldus(pdt, (u32)((tb * 2 + kch) * 2), logdt);
      ld1c<0>(ev, 0u, ws);
    }
    asm volatile("s_barrier" ::: "memory");
    init1();
#pragma unroll
    for (int i = 0; i < 4; ++i) dma(1, i);
    u32 ta = (u32)(L_TA + (v * 64 + l) * 128 + (((l & 1) | ((l >> 1) & 2) | ((l << 1) & 4)) * 16));
    u32 tbb = (u32)(L_TB + (v * 64 + l) * TB_P);
    PIN(ta); PIN(tbb);
    waitv<4>();
    PIN(ev); PIN(pA); PIN(pI); PIN(pR); PIN(pC); PIN(pR2); PIN(pC2); PIN(pdt);
    STAMP(1);
    tag = (u32)__builtin_amdgcn_readfirstlane(ev) + 1u;
    const float dt = rbf(__builtin_amdgcn_exp2f(bf2f(pdt) * 1.44269504089f));
    const float ar = -rbf(__builtin_amdgcn_exp2f(bf2f(pA) * 1.44269504089f)) * dt * 1.44269504089f;
    const float ai = bf2f(pI) * dt * 0.15915494309f;
    dma(1, 4);
    u32 ex = wmaxu(umax(umax(pR & 0x7f80u, pC & 0x7f80u), umax(pR2 & 0x7f80u, pC2 & 0x7f80u))) >> 7;
    ex = ex < 8u ? 8u : (ex > 250u ? 250u : ex);
    const float sc = u2f((260u - ex) << 23);
    const float unsc = u2f((ex - 6u) << 23) * 2.0f * dt;
    const cf C = {bf2f(pR) * sc, bf2f(pC) * sc};
    dma(1, 5);
    const cf z = cexp(ar, ai, 1.f), z8 = cexp(ar, -ai, 8.f);
    dma(1, 6);
    const cf W = cexp(ar, ai, 128.f);
    cf U[8], Y[8], Z[8], W2, s6, s7;
    {
      auto putA = [&](int chunk, const c2* p) INL {
        *LP(v4u, ta ^ (u32)(chunk * 16)) = (v4u){pkh2(p[0].r), pkh2(p[1].r), pkh2(p[2].r), pkh2(p[3].r)};
        *LP(v4u, ta ^ (u32)(chunk * 16 + 64)) = (v4u){pkh2(p[0].i), pkh2(p[1].i), pkh2(p[2].i), pkh2(p[3].i)};
      };
      c2 P[4], Q[4], R[4], S[4];
      const cf z2 = cmul(z, z), cz = cmul(C, z);
      W2 = cmul(W, W); Y[0] = {1.f, 0.f}; Y[1] = z8; Y[2] = cmul(z8, z8);
      dma(1, 7);
      P[0] = {(v2f){C.r, cz.r}, (v2f){C.i, cz.i}};
#pragma unroll
      for (int j = 1; j < 4; ++j) P[j] = mulS(P[j - 1], z2);
      putA(0, P);
#pragma unroll
      for (int j = 0; j < 4; ++j) Q[j] = mulS(P[j], W);
      putA(1, Q);
      R[0] = mulS({(v2f){Q[0].r[0], Q[1].r[0]}, (v2f){Q[0].i[0], Q[1].i[0]}}, W);
      R[1] = mulS({(v2f){Q[2].r[0], Q[3].r[0]}, (v2f){Q[2].i[0], Q[3].i[0]}}, W);
      R[2] = mulS(R[0], W); R[3] = mulS(R[1], W);
      putA(2, R);
      S[0] = mulS({(v2f){R[2].r[0], R[3].r[0]}, (v2f){R[2].i[0], R[3].i[0]}}, W);
#pragma unroll
      for (int j = 1; j < 4; ++j) S[j] = mulS(S[j - 1], W);
      putA(3, S);
      s6 = {S[2].r[0], S[2].i[0]}; s7 = {S[3].r[0], S[3].i[0]};
      PIN(s6.r); PIN(s6.i); PIN(s7.r); PIN(s7.i); PIN(W2.r); PIN(W2.i); PIN(Y[1].r); PIN(Y[1].i); PIN(Y[2].r); PIN(Y[2].i);
    }
    u32 lnt = (u32)(L_LN + q4 * 1088 + v * 256 + g * 32 + p4 * 8);
    PIN(lnt);
    STAMP(2);
    BETA(0);
    {
      const v8s t0 = cat(trread(lnt), trread(lnt + 4 * 1088)), t1 = cat(trread(lnt + 128), trread(lnt + 128 + 4 * 1088));
      st4c<0>(__builtin_bit_cast(v4u, t0), l16, ws + WS_XNB + tb * 8192 + v * 2048);
      st4c<1024>(__builtin_bit_cast(v4u, t1), l16, ws + WS_XNB + tb * 8192 + v * 2048);
    }
    const char* pfb = (const char*)w2 + (224 * (bi >> 2) + 2 * ((bi & 3) * 4 + v)) * 1024;
    auto pf = [&](int it) INL { if (PF_W2) touch(junk, (u32)((l >> 2) * 128), pfb + it * 32768); };
    pf(0);
    waitv<2 + PF_W2>();
    BETA(1);
    auto pack = [&](const cf* p, v4u& re, v4u& im) INL {
#pragma unroll
      for (int j = 0; j < 4; ++j) { re[j] = pkh(p[2 * j].r, p[2 * j + 1].r); im[j] = pkh(p[2 * j].i, p[2 * j + 1].i); }
      PIN(re); PIN(im);
    };
    v4u re, im;
    PIN(W2.r); PIN(Y[2].r);
    dma(3, 0); cmul2(U[0], s6, U[1], s7, W2);
    dma(3, 1); dma(3, 2); cmul2(U[2], U[0], U[3], U[1], W2);
    dma(3, 3); dma(3, 4); cmul2(U[4], U[2], U[5], U[3], W2);
    dma(3, 5); dma(3, 6); cmul2(U[6], U[4], U[7], U[5], W2);
    dma(3, 7); pf(1);
    PIN(U[7].r);
    BETA(2);
    {
      cf last, dum;
      dma(4, 0); pack(U, re, im);
      dma(4, 1); dma(4, 2); cmul2(last, U[6], dum, U[7], W2);
      dma(4, 3); dma(4, 4); cmul2(Y[3], Y[1], Y[4], Y[2], Y[2]);
      u32 l0 = pkh(last.r, 0.f), l1 = pkh(last.i, 0.f);
      dma(4, 5); dma(4, 6); cmul2(Y[5], Y[3], Y[6], Y[4], Y[2]);
      dma(4, 7); pf(2);
      *LP(v4u, tbb) = re; *LP(v4u, tbb + 48) = im; *LP(v4u, tbb + 96) = (v4u){l0, 0u, l1, 0u};
    }
    waitv<8 + 2 * PF_W2>(); BETA(3);
    if (tid == 256) st1c<0>(tag, 0u, ws + WS_FA + tb * 4);
    dma(5, 0); cmul2(Y[7], Y[5], Z[0], Y[6], Y[2]);
    dma(5, 1); dma(5, 2); pack(Y, re, im);
    dma(5, 3); dma(5, 4); cmul2(Z[1], Y[1], Z[2], Y[2], Z[0]);
    dma(5, 5); dma(5, 6); cmul2(Z[3], Y[3], Z[4], Y[4], Z[0]);
    dma(5, 7); pf(3);
    *LP(v4u, tbb + 16) = re; *LP(v4u, tbb + 64) = im;
    waitv<8 + 2 * PF_W2>(); BETA(4);
    const int kt = v & 1, ri = q4 & 1;
    u32 a0[2], a2, ay;
    {
      cf dum;
      dma(6, 0); cmul2(Z[5], Y[5], Z[6], Y[6], Z[0]);
      dma(6, 1); dma(6, 2); cmul2(Z[7], Y[7], dum, Y[7], Z[0]);
      dma(6, 3); dma(6, 4); pack(Z, re, im);
      dma(6, 5); dma(6, 6);
      const u32 rb = (u32)(L_TB + (kch * 128 + 4 * g + (q4 >> 1)) * TB_P);
      a2 = rb + (u32)(p4 < 2 ? ri * 48 + p4 * 8 : 96 + ri * 8);
      ay = rb + (u32)(ri * 48 + 16 + p4 * 8);
      PIN(a2); PIN(ay);
      dma(6, 7); pf(4);
      *LP(v4u, tbb + 32) = re; *LP(v4u, tbb + 80) = im;
    }
    waitv<8 + 2 * PF_W2>(); BETA(5);
    u32 ka[11];
    const int lagtop = 2047 - 8 * (tid & 255);
    const float lf = (float)lagtop;
    const bool big = lagtop >= 160;
    auto kaddr = [&](int e) INL {
      float f0 = lf - (float)e, f1 = lf - (float)(e + 1);
      OPQ(f0);
      const u32 pk = pkbf(f0, f1);
      ka[e] = (u32)(L_KD + 8 + 2 * (big ? (int)(pk & 0xffffu) - 17024 : lagtop - e));
      PIN(ka[e]);
      if (e + 1 < 11) { ka[e + 1] = (u32)(L_KD + 8 + 2 * (big ? (int)(pk >> 16) - 17024 : lagtop - e - 1)); PIN(ka[e + 1]); }
    };
    dma(7, 0); dma(7, 1);
    init2();
    dma(7, 2); dma(7, 3); dma(7, 4);
#pragma unroll
    for (int t = 0; t < 2; ++t) {
      const int n = 4 * g + 2 * t + (q4 >> 1);
      a0[t] = (u32)(L_TA + (kch * 128 + n) * 128 + (((ri * 4 + 2 * kt + (p4 >> 1)) ^ ((n & 1) | ((n >> 1) & 2) | ((n << 1) & 4))) * 16) + (p4 & 1) * 8);
      PIN(a0[t]);
    }
    dma(7, 5); dma(7, 6); dma(7, 7);
    pf(5);
    waitv<8 + 2 * PF_W2>(); BETA(6);
    v4u pollA, uv0, uv1; v2u gr;
    const char* grp = ws + (v < 2 ? WS_STAT + (mt1 * 128 + 64 * v) * 8 : WS_CS + (nt1 * 128 + 64 * (v - 2)) * 8);
    ld4c<0>(pollA, l16, ws + WS_FA);
    ld2c<0>(gr, (u32)(l * 8), grp);
    pf(6);
#pragma unroll
    for (int e = 0; e < 8; ++e) {
      SB0(); dma2(e < 4 ? w : v, 0, 2 * SLOT, e & 3); SB0();
      if (e & 1) kaddr(e - 1);
    }
    waitv<10 + 2 * PF_W2>(); BETA(7);
    const u32 kb = (u32)(L_KD + kch * KD_P + 8);
    u32 kw0, kw1, kc, ro, ua;
    v8s yb[8], xk[8], xj[8];
#pragma unroll
    for (int e = 0; e < 8; ++e) {
      SB0(); dma2(e < 4 ? w : v, 1, 0, e & 3); SB0();
      if (e == 0) kaddr(8);
      if (e == 1) kaddr(10);
      if (e == 2) {
        const int base = kt == 0 ? 128 * (g >> 1) + 4 * (g & 1) + 8 * c16 : (g < 2 ? 256 + 64 * g + 4 * c16 : 256 + 64 * g + 2 * c16);
        kw0 = kb + base * 2; kw1 = kw0 + ((kt == 1 && g >= 2) ? 64 : 4);
        PIN(kw0); PIN(kw1);
      }
      if (e == 3) {
        kc = kb + (u32)((512 + 64 * g + c16) * 2);
        ro = (u32)(L_R + (tid & 255) * 16); ua = (u32)(L_U + (tid & 1) * 4608 + 512 + ((tid & 255) >> 1) * 16);
        PIN(kc); PIN(ro); PIN(ua);
      }
      if (e >= 4) {
        xk[2 * e - 8] = cat(trread(a0[0] + (2 * e - 8) * 2048), trread(a0[1] + (2 * e - 8) * 2048));
        xk[2 * e - 7] = cat(trread(a0[0] + (2 * e - 7) * 2048), trread(a0[1] + (2 * e - 7) * 2048));
      }
    }
    waitv<16>(); PIN(pollA); PIN(gr); PIN(junk);
    if (__builtin_expect(!wall(gr[1] == tag && pollA[0] == tag && pollA[1] == tag && pollA[2] == tag && pollA[3] == tag), 0)) {
      for (u32 spins = 0; spins < kSpin; ++spins) {
        __builtin_amdgcn_s_sleep(4);
        ld4c<0>(pollA, l16, ws + WS_FA);
        ld2c<0>(gr, (u32)(l * 8), grp);
        waitv<0>(); PIN(pollA); PIN(gr);
        if (wall(gr[1] == tag && pollA[0] == tag && pollA[1] == tag && pollA[2] == tag && pollA[3] == tag)) break;
      }
    }
    *LP(u32, L_CSP + (tid & 255) * 4) = gr[0];
    BETA(8);
    __builtin_amdgcn_s_setprio(3);
    if (b == 0 && tid == 256) st1c<0>(tag, 0u, ws + WS_E);
    ld4c<0>(uv0, (u32)(((tid & 255) >> 1) * 8192 + (tid & 1) * 16), ws + WS_XNB + tb * 32);
    ld4c<0>(uv1, (u32)(((tid & 255) >> 1) * 8192 + (tid & 1) * 16), ws + WS_XNB + tb * 32 + 128 * 8192);
    {
      v4f k0 = {0.f, 0.f, 0.f, 0.f}, k2 = k0;
#pragma unroll
      for (int s = 0; s < 8; ++s) {
        yb[s] = cat(trread(ay + s * 16 * TB_P), trread(ay + s * 16 * TB_P + 2 * TB_P));
        xj[s] = cat(trread(a2 + s * 16 * TB_P), trread(a2 + s * 16 * TB_P + 2 * TB_P));
        SB0(); dma2(s < 4 ? w : v, 2, SLOT, s & 3); SB0();
        if (s >= 2) { k0 = mfma_h(xk[s - 2], yb[s - 2], k0); k2 = mfma_h(xj[s - 2], yb[s - 2], k2); }
      }
#pragma unroll
      for (int s = 6; s < 8; ++s) { k0 = mfma_h(xk[s], yb[s], k0); k2 = mfma_h(xj[s], yb[s], k2); }
      waitv<14>();
      w2read(0, 2 * SLOT); w2read(1, 0);
      *LP(u32, kw0) = pkh(k0[0], k0[1]); *LP(u32, kw1) = pkh(k0[2], k0[3]);
      const u32 k01 = pkh(k2[0], k2[1]), k23 = pkh(k2[2], k2[3]);
      if (g < 3) {
        *LP(u16, kc) = (u16)k01; *LP(u16, kc + 32) = (u16)(k01 >> 16);
        *LP(u16, kc + 64) = (u16)k23; *LP(u16, kc + 96) = (u16)(k23 >> 16);
      }
      if (l == 0) { *LP(v2u, kb - 8) = (v2u){0u, 0u}; *LP(float, L_US + kch * 4) = unsc; }
    }
    __builtin_amdgcn_s_setprio(1);
    STAMP(21);
    bar();
    u32 kv[2][11];
#pragma unroll
    for (int e = 0; e < 12; ++e) {
      SB0();
      if (e < 4) dma2(w, 3, 2 * SLOT, e); else if (e < 8) dma2(v, 3, L_W3, e & 3); else dma2(w, 4, 0, e & 3);
      SB0();
      if (e < 11) { kv[0][e] = *LP(u16, ka[e]); kv[1][e] = *LP(u16, ka[e] + KD_P); }
    }
    waitv<16>(); PIN(uv0); PIN(uv1);
    w2read(2, SLOT);
    auto expand = [&](int ch) INL {
      u32 pr[10];
#pragma unroll
      for (int e = 0; e < 10; ++e) pr[e] = kv[ch][e] | (kv[ch][e + 1] << 16);
#pragma unroll
      for (int sg = 0; sg < 4; ++sg)
        *LP(v4u, ro + (ch * 4 + sg) * 4160) = (v4u){pr[3 - sg], pr[5 - sg], pr[7 - sg], pr[9 - sg]};
    };
    waitl(); stage2(5, SLOT);
    expand(0);
    expand(1);
    *LP(v4u, ua) = uv0; *LP(v4u, ua + 2048) = uv1;
    {
      const int i = tid & 255;
      if (i < 16) *LP(v4u, L_R + (i >> 1) * 4160 + 4096 + (i & 1) * 16) = (v4u){0u, 0u, 0u, 0u};
      if (i >= 64 && i < 128) *LP(v4u, L_U + ((i >> 5) & 1) * 4608 + (i & 31) * 16) = (v4u){0u, 0u, 0u, 0u};
    }
    waitv<8>();
    w2read(3, 2 * SLOT);
    waitl(); stage2(6, 2 * SLOT);
    __builtin_amdgcn_s_setprio(0);
    STAMP(22);
    bar();
    STAMP(23);
  }
  v4u hf[4][7];
  const char* hsrc = ws + WS_H + (tt2 * 4 * 56 + 7 * w) * 1024 + 3072;
  auto hld = [&](int ti, int j) INL {
    const char* p = hsrc + ((ti + ROT2 * dt2) & 3) * 56 * 1024;
    if (j == 0) ld4c<-3072>(hf[ti][0], l16, p);
    if (j == 1) ld4c<-2048>(hf[ti][1], l16, p);
    if (j == 2) ld4c<-1024>(hf[ti][2], l16, p);
    if (j == 3) ld4c<0>(hf[ti][3], l16, p);
    if (j == 4) ld4c<1024>(hf[ti][4], l16, p);
    if (j == 5) ld4c<2048>(hf[ti][5], l16, p);
    if (j == 6) ld4c<3072>(hf[ti][6], l16, p);
  };
  const char* fsrc = ws + WS_FH + (tt2 * 32 + ((7 * w) >> 1)) * 128;
  const u32 foff = (u32)((l & 3) * 128);
  const int gt0 = ((w >> 2) + ROT2 * dt2) & 3, gt1 = gt0 ^ 2;
  const u32 ooff = (u32)((((w & 3) * 4 + g) * D + c16 * 4) * 2);
  u32 pollH, dsp; v2u xv0, xv1;
  auto spinH = [&](const char* p) INL {
    if (__builtin_expect(!wall(pollH == tag), 0)) {
      for (u32 spins = 0; spins < kSpin; ++spins) {
        ld1c<0>(pollH, foff, p);
        waitv<0>(); PIN(pollH);
        if (wall(pollH == tag)) break;
        __builtin_amdgcn_s_sleep(2);
      }
    }
  };
  {
    const int ch = w >> 2;
    v4f ya[8];
    const v4f zero4 = {0.f, 0.f, 0.f, 0.f};
    u32 fb = (u32)(L_R + (ch * 4 + (c16 & 3)) * 4160 + (2028 - 32 * 57 - 64 * v + 8 * g - 4 * q4) * 2);
    u32 gb = (u32)(L_U + ch * 4608 + 512 + (16 * c16 - 16 + 8 * g - 64 * v - 32) * 2);
    OPQ(fb); OPQ(gb);
    auto ldF = [&](int qq, int i) INL { const u32 a = fb + (1 - qq) * 64 + (7 - i) * 512; return cat(*(volatile LAS v4s*)LP(v4s, a), *(volatile LAS v4s*)LP(v4s, a + 8)); };
    auto ldG = [&](int qq, int i) INL { return *LP(v8s, gb + (1 - qq) * 64 + i * 512); };
    const u32 pw = (u32)(w * 4096 + (g * 20 + c16) * 16);
    auto pwr = [&](int n) INL { *LP(v4f, pw + (n / 3) * SLOT + (n % 3) * 1280) = ya[n]; };
    v8s Gf[8], Fq[3], F2[8], G2[8];
    Fq[0] = ldF(0, 0);
#pragma unroll
    for (int i = 0; i < 8; ++i) Gf[i] = ldG(0, i);
    Fq[1] = ldF(0, 1);
#pragma unroll
    for (int i = 0; i < 8; ++i) {
      if (i + 2 < 8) Fq[(i + 2) % 3] = ldF(0, i + 2);
      SB0();
#pragma unroll
      for (int n = i; n < 8; ++n) {
        ya[n] = mfma_h(Fq[i % 3], Gf[n - i], i == 0 ? zero4 : ya[n]);
        const int m = i * 8 - i * (i - 1) / 2 + (n - i);
        if (m >= 8 && m < 20) {
          SB0();
          const int k = m - 8;
          if (w < 4) dma2(w, 4 + (k >> 2), (k >> 2) * SLOT, k & 3);
          SB0();
        }
      }
      SB0();
      if (i == 0) {
        waitl();
        ld2<0>(xv0, ooff, (const char*)x + ((tt2 * 64 + gt0 * 16) * D + dt2 * 64) * 2);
        ld2<0>(xv1, ooff, (const char*)x + ((tt2 * 64 + gt1 * 16) * D + dt2 * 64) * 2);
        ld1c<0>(dsp, 0u, (const char*)dskip + tb * 4);
      }
      if (i >= 3 && i < 7) { F2[i - 3] = ldF(1, i - 3); G2[i - 3] = ldG(1, i - 3); }
      if (i == 4) ld1c<0>(pollH, foff, fsrc);
    }
    STAMP(24);
    auto w2rd = [&](int j, u32 slotb, int dj) INL { wf[j][dj] = cat(trread(ra[dj] + slotb), trread(ra[dj] + slotb + 1024)); };
    auto tile = [&](int n) INL {
      if (n >= 2 && n + 2 < 8) { F2[n + 2] = ldF(1, n + 2); G2[n + 2] = ldG(1, n + 2); }
      SB0();
#pragma unroll
      for (int i = 0; i <= n; ++i) {
        ya[n] = mfma_h(F2[i], G2[n - i], ya[n]);
        const int m = n * (n + 1) / 2 + i - 10;
        SB0();
        if (m >= 0 && m < 12) w2rd(4 + (m >> 2), (m >> 2) * SLOT, m & 3);
        if (m >= 0 && (m & 1) == 0) hld(m / 14, (m >> 1) % 7);
        if (m >= 13 && (m & 1) == 1) pwr((m - 13) / 2);
        SB0();
      }
    };
#pragma unroll
    for (int n = 0; n < 4; ++n) tile(n);
    waitv<0>(); PIN(pollH); PIN(xv0); PIN(xv1); PIN(dsp);
    spinH(fsrc);
    STAMP(25);
#pragma unroll
    for (int n = 4; n < 8; ++n) tile(n);
    hld(1, 6);
    pwr(7);
  }
  bar();
  STAMP(26);
  {
    u32 pa = (u32)((w / 3) * SLOT + (w % 3) * 1280 + ((l & 3) * 20 + (l >> 2)) * 16);
    OPQ(pa);
    v4f rp[8];
#pragma unroll
    for (int k = 0; k < 8; ++k) rp[k] = *LP(v4f, pa + k * 4096);
    u32 uo = (u32)(L_U + 512 + tid * 8);
    OPQ(uo);
    const v4h u0 = *LP(v4h, uo), u1 = *LP(v4h, uo + 4608);
    const v2f us = *LP(v2f, L_US);
    {
      const float ds0 = bf2f(dsp), ds1 = bfhi(dsp);
      u32 o[4];
#pragma unroll
      for (int j = 0; j < 4; ++j)
        o[j] = pkbf(fma1(sum4(rp[0][j], rp[1][j], rp[2][j], rp[3][j]), us[0], mul1((float)u0[j], ds0)),
                    fma1(sum4(rp[4][j], rp[5][j], rp[6][j], rp[7][j]), us[1], mul1((float)u1[j], ds1)));
      char* sdst = ws + WS_SG + (w * 4 * 256 + tb) * 512;
      const u32 so = (u32)(g * 256 * 512 + c16 * 16);
      st4c<0>((v4u){o[0], tag, o[1], tag}, so, sdst);
      st4c<256>((v4u){o[2], tag, o[3], tag}, so, sdst);
    }
    STAMP(27);
    const u32 qw = (u32)(w * 4096 + c16 * 256);
    v4u sg0, sg1;
    const char* ssrc = ws + WS_SG + (tt2 * 256 + dt2 * 32) * 512;
    v4f a2[2][4];
    auto qbase = [&](int ti) INL { return ti == 0 ? (u32)L_R : (u32)((ti - 1) * SLOT); };
    auto qwr = [&](int ti, int dj) INL { *LP(v4f, qbase(ti) + qw + (((dj * 4 + g) ^ (c16 & 7)) * 16)) = a2[ti & 1][dj]; };
    const int rt = (w & 3) * 4 + g;
    auto redld = [&](u32 qb) INL {
      u32 a = qb + (u32)(rt * 256 + ((c16 ^ (rt & 7)) * 16));
      OPQ(a);
#pragma unroll
      for (int k = 0; k < 8; ++k) rp[k] = *LP(v4f, a + k * 4096);
    };
    auto fin = [&](v4f gg, v2u ss, v2u xx) INL {
      v2u o;
#pragma unroll
      for (int j = 0; j < 2; ++j)
        o[j] = pkbf(fma1(bf2f(ss[j]), gg[2 * j], bf2f(xx[j])), fma1(bfhi(ss[j]), gg[2 * j + 1], bfhi(xx[j])));
      return o;
    };
    GAS char* ob = (GAS char*)out + (tt2 * 64 * D + dt2 * 64) * 2;
    v4f ga; v2u sa0, sa1;
    auto g2 = [&](int ti) INL {
      const v4f zero4 = {0.f, 0.f, 0.f, 0.f};
#pragma unroll
      for (int j = 0; j < 7; ++j) {
        if (ti == 0) waitv<15>();
        if (ti == 1) waitv<15>();
        if (ti == 2) { if (j == 0) waitv<15>(); if (j == 1) waitv<14>(); if (j == 2) waitv<13>(); if (j == 3) waitv<12>(); if (j == 4) waitv<11>(); if (j == 5) waitv<10>(); if (j == 6) waitv<9>(); }
        PIN(hf[ti][j]);
#pragma unroll
        for (int dj = 0; dj < 4; ++dj) {
          a2[ti & 1][dj] = mfma_bf(wf[j][dj], __builtin_bit_cast(v8s, hf[ti][j]), j == 0 ? zero4 : a2[ti & 1][dj]);
          SB0();
          if (dj == 3 && ti < 2) hld(ti + 2, j);
          if (dj == 1 && ti >= 1 && j >= 1 && j < 5) qwr(ti - 1, j - 1);
          if (ti == 3 && j >= 1 && j < 5 && dj == 0) { ga[j - 1] = sum4(rp[0][j - 1], rp[1][j - 1], rp[2][j - 1], rp[3][j - 1]) + sum4(rp[4][j - 1], rp[5][j - 1], rp[6][j - 1], rp[7][j - 1]); PIN(ga); }
          if (ti == 3 && j >= 1 && j < 5 && dj == 3) { ga[j - 1] = silu(ga[j - 1]); PIN(ga); }
          if (ti == 3 && j == 5 && dj == 3) *(GAS v2u*)(ob + gt0 * 16 * D * 2 + ooff) = fin(ga, sa0, xv0);
          SB0();
        }
      }
    };
    g2(0);
    g2(1);
    ld4c<0>(sg0, (u32)(tid * 16), ssrc); ld4c<0>(sg1, (u32)(tid * 16), ssrc + 8192);
    bar();
    g2(2);
    waitv<0>(); PIN(sg0); PIN(sg1);
    if (__builtin_expect(!wall(sg0[1] == tag && sg0[3] == tag && sg1[1] == tag && sg1[3] == tag), 0)) {
      for (u32 spins = 0; spins < kSpin; ++spins) {
        ld4c<0>(sg0, (u32)(tid * 16), ssrc); ld4c<0>(sg1, (u32)(tid * 16), ssrc + 8192);
        waitv<0>(); PIN(sg0); PIN(sg1);
        if (wall(sg0[1] == tag && sg0[3] == tag && sg1[1] == tag && sg1[3] == tag)) break;
        __builtin_amdgcn_s_sleep(2);
      }
    }
    {
      u32 sa = (u32)(L_S + (2 * c16 + (g & 1)) * 136 + (tid >> 5) * 4);
      OPQ(sa);
      *LP(u32, sa) = sg0[0]; *LP(u32, sa + 4480) = sg0[2];
      *LP(u32, sa + 64) = sg1[0]; *LP(u32, sa + 64 + 4480) = sg1[2];
    }
    bar();
    STAMP(28);
    redld((w >> 2) ? qbase(1) : qbase(0));
    {
      const u32 sr = (u32)(L_S + (rt >> 1) * 136 + (rt & 1) * 4480 + c16 * 8);
      sa0 = *LP(v2u, sr + gt0 * 8 * 136); sa1 = *LP(v2u, sr + gt1 * 8 * 136);
    }
    g2(3);
#pragma unroll
    for (int dj = 0; dj < 4; ++dj) qwr(3, dj);
    bar();
    STAMP(29);
    redld((w >> 2) ? qbase(3) : qbase(2));
    {
      const v4f sum = ((rp[0] + rp[1]) + (rp[2] + rp[3])) + ((rp[4] + rp[5]) + (rp[6] + rp[7]));
      *(GAS v2u*)(ob + gt1 * 16 * D * 2 + ooff) = fin((v4f){silu(sum[0]), silu(sum[1]), silu(sum[2]), silu(sum[3])}, sa1, xv1);
    }
  }
  STAMP(30);
}

extern "C" void kernel_launch(void* const* d_in, const int* in_sizes, int n_in, void* d_out, int out_size,
                              void* d_ws, size_t ws_size, hipStream_t stream) {
  fused_kernel<<<256, 512, 0, stream>>>((const u16*)d_in[0], (const u16*)d_in[1], (const u16*)d_in[2],
                                        (const u16*)d_in[3], (const u16*)d_in[4], (const u16*)d_in[5],
                                        (const u16*)d_in[6], (const u16*)d_in[8], (const u16*)d_in[9],
                                        (u16*)d_out, (char*)d_ws);
}
```

```cpp
#include <hip/hip_runtime.h>
#include <stdint.h>

typedef unsigned short u16;
typedef unsigned int u32;
typedef unsigned long long u64;
typedef u32 v4u __attribute__((ext_vector_type(4)));
typedef u32 v2u __attribute__((ext_vector_type(2)));
typedef float v4f __attribute__((ext_vector_type(4)));
typedef float v2f __attribute__((ext_vector_type(2)));
typedef float v16f __attribute__((ext_vector_type(16)));
typedef short v8s __attribute__((ext_vector_type(8)));
typedef short v4s __attribute__((ext_vector_type(4)));
typedef __bf16 v8b __attribute__((ext_vector_type(8)));
typedef __bf16 v2b __attribute__((ext_vector_type(2)));
typedef _Float16 v8h __attribute__((ext_vector_type(8)));
typedef _Float16 v4h __attribute__((ext_vector_type(4)));
typedef _Float16 v2h __attribute__((ext_vector_type(2)));
#define LAS __attribute__((address_space(3)))
#define GAS __attribute__((address_space(1)))
#define DEVI __device__ __forceinline__
#define INL __attribute__((always_inline))

#define PF_CODE 1
#define PF_CODE_BYTES 27904
#define PF_W2 0
#define ROT1 2
#define ROT2 1
#define PF_MALL 1
#define NTP " nt"
#define POLLI 5
#define FLAGEARLY 1
#define CKT 5
#define POLLM 0
#define CPRIO 1
#define GAM 2
#define BPRI 1
#define LNNT 1
#define C1A 1
#define OUTMOD " nt"
#define XPF 0
#define PDEC 1
#define KLATE 1
#define FULLF 1
#define C2A 4
#define FLA 2
#define SGS 2
#define PAS 4
#define XGS 0
#define STAMPS 0

constexpr int L = 2048, D = 512, NS = 128, F = 1792;
constexpr int WS_E = 0, WS_FA = 1024, WS_STAT = 8192, WS_CS = 40960;
constexpr int WS_XNB = 65536, WS_SG = WS_XNB + (2 << 20), WS_H = WS_SG + (4 << 20), WS_FH = WS_H + L * F * 2, WS_XG = WS_FH + (256 << 10);
constexpr int WS_STAMP = 24 << 20;
constexpr int SLOT = 32768, AUX = 3 * SLOT;
constexpr int L_TA = AUX, L_TB = AUX + 32768, TB_P = 112;
constexpr int L_LN = L_TB;
constexpr int L_KD = AUX + 61440, KD_P = 1488, L_US = L_KD + 2976, L_CSP = L_KD + 3072;
constexpr int L_R = AUX, L_U = AUX + 33280, L_S = AUX + 42496, L_W3 = AUX + 43008, L_X = AUX + 51712;
constexpr unsigned kSpin = 1u << 20;

__shared__ __attribute__((aligned(16))) char smem[163840];
#define LP(T, off) ((LAS T*)((LAS char*)smem + (off)))
#define PIN(x) asm volatile("" : "+v"(x))
#define OPQ(x) asm("" : "+v"(x))
#define SB0() __builtin_amdgcn_sched_barrier(0)

DEVI float u2f(u32 v) { return __builtin_bit_cast(float, v); }
DEVI u32 f2u(float v) { return __builtin_bit_cast(u32, v); }
DEVI bool wall(bool p) { return __builtin_amdgcn_ballot_w64(!p) == 0; }
DEVI float bf2f(u32 v) { return u2f(v << 16); }
DEVI float bfhi(u32 v) { return u2f(v & 0xffff0000u); }
DEVI u32 pkbf(float a, float b) { v2f v = {a, b}; return __builtin_bit_cast(u32, __builtin_convertvector(v, v2b)); }
DEVI u32 pkh(float a, float b) { v2f v = {a, b}; return __builtin_bit_cast(u32, __builtin_convertvector(v, v2h)); }
DEVI float rbf(float a) { return bf2f(pkbf(a, a)); }
DEVI float silu(float v) { return v * __builtin_amdgcn_rcpf(1.0f + __builtin_amdgcn_exp2f(v * -1.44269504089f)); }
DEVI void waitl() { asm volatile("" ::: "memory"); __builtin_amdgcn_s_waitcnt(0xC07F); asm volatile("" ::: "memory"); }
DEVI void bar() { waitl(); asm volatile("s_barrier" ::: "memory"); }
template <int N> DEVI void waitv() { asm volatile("s_waitcnt vmcnt(%0)" ::"n"(N) : "memory"); }
typedef __amdgpu_buffer_rsrc_t rsrc_t;
DEVI rsrc_t mkrs(const void* p, u32 n) { return __builtin_amdgcn_make_buffer_rsrc((void*)p, 0, n, 0x00020000); }
template <int O> DEVI void glds2(rsrc_t rs, u32 so, u32 va, u32 vb, u32 m) {
  asm volatile("s_mov_b32 m0, %4\n\ts_nop 0\n\tbuffer_load_dwordx4 %0, %2, %3 offen offset:%5 lds\n\tbuffer_load_dwordx4 %1, %2, %3 offen offset:%6 lds"
               ::"v"(va), "v"(vb), "s"(rs), "s"(so), "s"(m), "n"(O), "n"(O + 1024) : "memory");
}
template <int O> DEVI void glds1(rsrc_t rs, u32 so, u32 va, u32 m) {
  asm volatile("s_mov_b32 m0, %3\n\ts_nop 0\n\tbuffer_load_dwordx4 %0, %1, %2 offen offset:%4 lds"
               ::"v"(va), "s"(rs), "s"(so), "s"(m), "n"(O) : "memory");
}
template <int O> DEVI void ld4nt(v4u& d, u32 voff, const void* sb) { asm volatile("global_load_dwordx4 %0, %1, %2 offset:%3 nt" : "=v"(d) : "v"(voff), "s"(sb), "n"(O) : "memory"); }
template <int O> DEVI void bst4c(v4u d, u32 voff, rsrc_t rs, u32 so) { asm volatile("buffer_store_dwordx4 %0, %1, %2, %3 offen offset:%4 sc1\n\ts_nop 1" ::"v"(d), "v"(voff), "s"(rs), "s"(so), "n"(O) : "memory"); }
template <int O> DEVI void bst1c(u32 d, rsrc_t rs, u32 so) { asm volatile("buffer_store_dword %0, off, %1, %2 offset:%3 sc1" ::"v"(d), "s"(rs), "s"(so), "n"(O) : "memory"); }
template <int O> DEVI void ld4(v4u& d, u32 voff, const void* sb) { asm volatile("global_load_dwordx4 %0, %1, %2 offset:%3" : "=v"(d) : "v"(voff), "s"(sb), "n"(O) : "memory"); }
template <int O> DEVI void ld4c(v4u& d, u32 voff, const void* sb) { asm volatile("global_load_dwordx4 %0, %1, %2 offset:%3 sc1" : "=v"(d) : "v"(voff), "s"(sb), "n"(O) : "memory"); }
template <int O> DEVI void ld2(v2u& d, u32 voff, const void* sb) { asm volatile("global_load_dwordx2 %0, %1, %2 offset:%3" : "=v"(d) : "v"(voff), "s"(sb), "n"(O) : "memory"); }
template <int O> DEVI void ld2c(v2u& d, u32 voff, const void* sb) { asm volatile("global_load_dwordx2 %0, %1, %2 offset:%3 sc1" : "=v"(d) : "v"(voff), "s"(sb), "n"(O) : "memory"); }
template <int O> DEVI void ld1c(u32& d, u32 voff, const void* sb) { asm volatile("global_load_dword %0, %1, %2 offset:%3 sc1" : "=v"(d) : "v"(voff), "s"(sb), "n"(O) : "memory"); }
DEVI void touch(u32& d, u32 voff, const void* sb) { asm volatile("global_load_dword %0, %1, %2" : "+v"(d) : "v"(voff), "s"(sb) : "memory"); }
DEVI void ldus(u32& d, u32 voff, const void* sb) { asm volatile("global_load_ushort %0, %1, %2" NTP : "=v"(d) : "v"(voff), "s"(sb) : "memory"); }
template <int O> DEVI void st4c(v4u d, u32 voff, void* sb) { asm volatile("global_store_dwordx4 %0, %1, %2 offset:%3 sc1\n\ts_nop 1" ::"v"(voff), "v"(d), "s"(sb), "n"(O) : "memory"); }
template <int O> DEVI void st2c(v2u d, u32 voff, void* sb) { asm volatile("global_store_dwordx2 %0, %1, %2 offset:%3 sc1" ::"v"(voff), "v"(d), "s"(sb), "n"(O) : "memory"); }
template <int O> DEVI void st2o(v2u d, u32 voff, void* sb) { asm volatile("global_store_dwordx2 %0, %1, %2 offset:%3" OUTMOD ::"v"(voff), "v"(d), "s"(sb), "n"(O) : "memory"); }
template <int O> DEVI void st1c(u32 d, u32 voff, void* sb) { asm volatile("global_store_dword %0, %1, %2 offset:%3 sc1" ::"v"(voff), "v"(d), "s"(sb), "n"(O) : "memory"); }

DEVI v4s trread(u32 off) { return __builtin_amdgcn_ds_read_tr16_b64_v4i16(LP(v4s, off)); }
DEVI v8s cat(v4s a, v4s b) { return __builtin_shufflevector(a, b, 0, 1, 2, 3, 4, 5, 6, 7); }
DEVI v4f mfma_bf(v8s a, v8s b, v4f c) {
  return __builtin_amdgcn_mfma_f32_16x16x32_bf16(__builtin_bit_cast(v8b, a), __builtin_bit_cast(v8b, b), c, 0, 0, 0);
}
DEVI v16f mfma32(v8s a, v8s b, v16f c) {
  return __builtin_amdgcn_mfma_f32_32x32x16_bf16(__builtin_bit_cast(v8b, a), __builtin_bit_cast(v8b, b), c, 0, 0, 0);
}
DEVI v4f mfma_h(v8s a, v8s b, v4f c) {
  return __builtin_amdgcn_mfma_f32_16x16x32_f16(__builtin_bit_cast(v8h, a), __builtin_bit_cast(v8h, b), c, 0, 0, 0);
}
template <int C> DEVI float dpp(float v) { return u2f((u32)__builtin_amdgcn_update_dpp(0, (int)f2u(v), C, 0xf, 0xf, true)); }
DEVI float sum16x(float v) {
  auto a = __builtin_amdgcn_permlane16_swap(f2u(v), f2u(v), false, false);
  return u2f(a[0]) + u2f(a[1]);
}
DEVI float sum32x(float v) {
  auto a = __builtin_amdgcn_permlane32_swap(f2u(v), f2u(v), false, false);
  return u2f(a[0]) + u2f(a[1]);
}
DEVI float wsum(float v) {
  v += dpp<0xB1>(v); v += dpp<0x4E>(v); v += dpp<0x141>(v); v += dpp<0x140>(v);
  return sum32x(sum16x(v));
}
template <int C> DEVI u32 dppu(u32 v) { return (u32)__builtin_amdgcn_update_dpp(0, (int)v, C, 0xf, 0xf, true); }
DEVI u32 umax(u32 a, u32 b) { return a > b ? a : b; }
DEVI u32 wmaxu(u32 v) {
  v = umax(v, dppu<0xB1>(v)); v = umax(v, dppu<0x4E>(v)); v = umax(v, dppu<0x141>(v)); v = umax(v, dppu<0x140>(v));
  auto a = __builtin_amdgcn_permlane16_swap(v, v, false, false);
  v = umax(a[0], a[1]);
  auto b = __builtin_amdgcn_permlane32_swap(v, v, false, false);
  return umax(b[0], b[1]);
}
struct cf { float r, i; };
DEVI cf cmul(cf a, cf b) { return {a.r * b.r - a.i * b.i, a.r * b.i + a.i * b.r}; }
DEVI void cmul2(cf& o0, cf a0, cf& o1, cf a1, cf b) {
  asm("v_mul_f32 %0, %5, %9\n\tv_mul_f32 %1, %4, %9\n\tv_mul_f32 %2, %7, %9\n\tv_mul_f32 %3, %6, %9\n\t"
      "v_fma_f32 %0, %4, %8, -%0\n\tv_fmac_f32 %1, %5, %8\n\tv_fma_f32 %2, %6, %8, -%2\n\tv_fmac_f32 %3, %7, %8"
      : "=&v"(o0.r), "=&v"(o0.i), "=&v"(o1.r), "=&v"(o1.i) : "v"(a0.r), "v"(a0.i), "v"(a1.r), "v"(a1.i), "v"(b.r), "v"(b.i));
}
struct c2 { v2f r, i; };
DEVI c2 mulS(c2 a, cf s) { return {a.r * s.r - a.i * s.i, a.r * s.i + a.i * s.r}; }
DEVI u32 pkh2(v2f v) { return __builtin_bit_cast(u32, __builtin_convertvector(v, v2h)); }
DEVI u32 pkbf2(v2f v) { return __builtin_bit_cast(u32, __builtin_convertvector(v, v2b)); }
DEVI cf cexp(float ar, float ai, float p) {
  float e = __builtin_amdgcn_exp2f(ar * p);
  float th = __builtin_amdgcn_fractf(ai * p);
  return {e * __builtin_amdgcn_cosf(th), e * __builtin_amdgcn_sinf(th)};
}
#define STAMP(i)
#define BETA(i) bar()
#define ESTAMP(i)
#define PADINIT()

#define HPOS {5,7,9,11,13,15,17, 19,21,23,25,29,30,30, 43,47,51,55,59,63,67, 71,75,79,83,87,91,95}
#define STPOS 31
#define SGPOS 101
#define XGPOS 143
constexpr int XS0 = 110, XS1 = 114;
struct Sched { int h[28]; int wait[28]; };
constexpr Sched mk_sched() {
  Sched t = {HPOS, {}};
  for (int k = 0; k < 28; ++k) {
    int n = 0;
    const int use = 40 + 4 * k;
    for (int j = 0; j < 28; ++j) if ((t.h[j] > t.h[k] || (t.h[j] == t.h[k] && j > k)) && t.h[j] < use) ++n;
    if (STPOS >= t.h[k] && STPOS < use) n += 2;
    if (SGPOS >= t.h[k] && SGPOS < use) n += 2;
    if (XGPOS >= t.h[k] && XGPOS < use) n += 2;
    if (XS0 >= t.h[k] && XS0 < use) n += 1;
    if (XS1 >= t.h[k] && XS1 < use) n += 1;
    t.wait[k] = n;
  }
  return t;
}
constexpr Sched SCH = mk_sched();
constexpr int sgwait() { int n = (XS0 > SGPOS ? 1 : 0) + (XS1 > SGPOS ? 1 : 0); for (int j = 0; j < 28; ++j) if (SCH.h[j] > SGPOS) ++n; return n; }
DEVI void waitvn(int n) { asm volatile("s_waitcnt vmcnt(%0)" ::"i"(n) : "memory"); }
struct G1F { v8s x[4][2], w[4][2]; };
struct G1 {
  v16f acc[2][2];
  G1F fa, fb;
  u32 xo[4], wo[2];
};
DEVI float sum4(float a, float b, float c, float d) {
  float r, t;
  asm("v_add_f32 %0, %2, %3\n\tv_add_f32 %1, %4, %5\n\tv_add_f32 %0, %0, %1" : "=&v"(r), "=&v"(t) : "v"(a), "v"(b), "v"(c), "v"(d));
  return r;
}
DEVI float fma1(float a, float b, float c) { float r; asm("v_fma_f32 %0, %1, %2, %3" : "=v"(r) : "v"(a), "v"(b), "v"(c)); return r; }
DEVI float mul1(float a, float b) { float r; asm("v_mul_f32 %0, %1, %2" : "=v"(r) : "v"(a), "v"(b)); return r; }
template <int O> DEVI void gldsn(rsrc_t rs, u32 so, u32 va) {
  asm volatile("buffer_load_dwordx4 %0, %1, %2 offen offset:%3 lds" ::"v"(va), "s"(rs), "s"(so), "n"(O) : "memory");
}
template <bool FIRST, bool MMA, bool READ> DEVI void g1_step(G1& s, G1F& c, G1F& n, u32 sb) {
  const v16f zero = {0.f, 0.f, 0.f, 0.f, 0.f, 0.f, 0.f, 0.f, 0.f, 0.f, 0.f, 0.f, 0.f, 0.f, 0.f, 0.f};
  const u32 w0 = sb + s.wo[0], w1 = sb + s.wo[1];
#pragma unroll
  for (int m = 0; m < 16; ++m) {
    const int k = m >> 2, tb = (m >> 1) & 1, fb = m & 1;
    if (MMA) s.acc[tb][fb] = mfma32(c.w[k][fb], c.x[k][tb], (FIRST && k == 0) ? zero : s.acc[tb][fb]);
    if (READ && m < 8) {
      const int kr = m >> 1;
      if ((m & 1) == 0) {
        n.x[kr][0] = *LP(v8s, sb + s.xo[kr]);
        n.w[kr][0] = cat(trread(w0 + kr * 4096), trread(w0 + kr * 4096 + 1024));
      } else {
        n.w[kr][1] = cat(trread(w1 + kr * 4096), trread(w1 + kr * 4096 + 1024));
        n.x[kr][1] = *LP(v8s, sb + s.xo[kr] + 4096);
      }
      SB0();
    }
  }
  if (READ) SB0();
}

__global__ __launch_bounds__(512, 2) void fused_kernel(
    const u16* x, const u16* logA, const u16* Aim, const u16* Cre, const u16* Cim, const u16* logdt,
    const u16* dskip, const u16* w1, const u16* w2, u16* out, char* ws) {
  PADINIT();
  u32 junk = 0;
  if (__builtin_amdgcn_workgroup_id_x() < 32) {
    const u32 o = __builtin_amdgcn_workitem_id_x() * 128;
    if (o < PF_CODE_BYTES) touch(junk, o, (const char*)(__builtin_amdgcn_s_getpc() & ~255ull));
  }
  asm volatile("" : "+s"(x), "+s"(logA), "+s"(Aim), "+s"(Cre), "+s"(Cim), "+s"(logdt), "+s"(dskip), "+s"(w1), "+s"(w2), "+s"(out), "+s"(ws));
  const int tid = __builtin_amdgcn_workitem_id_x(), l = tid & 63;
  int w = __builtin_amdgcn_readfirstlane(tid >> 6);
  const int b = __builtin_amdgcn_workgroup_id_x(), xcd = b & 7, bi = b >> 3;
  const int g = l >> 4, c16 = l & 15, q4 = c16 >> 2, p4 = l & 3, v = w & 3;
  STAMP(0);

  const bool hasg1 = bi >= 4;
  const int mt1 = (xcd >> 1) * 4 + (bi & 3), nt1 = (xcd & 1) * 7 + (hasg1 ? (bi >> 2) - 1 : 0);
  int kh = xcd & 1, mt2 = mt1, dt2 = bi >> 2;
  int tb = xcd * 32 + bi;
  const u32 nrec = hasg1 ? 0x7fffffffu : 0u;
  const u32 l16 = (u32)(l * 16);

  u32 vo2[4], ra[4];
  rsrc_t rs2, rsX, rsW;
  auto init2 = [&](bool ld) INL {
    const u16* p = w2;
    asm volatile("" : "+s"(p));
    rs2 = mkrs((const char*)p + (896 * kh + 224 * v) * 1024 + dt2 * 128, 224 * 1024 - dt2 * 128);
    const int row = 16 * (g >> 1) + 4 * (g & 1) + q4;
#pragma unroll
    for (int i = 0; i < 4; ++i) {
      ra[i] = (u32)(v * 4096 + row * 128 + ((i ^ ((row >> 1) & 3)) * 32) + p4 * 8);
      PIN(ra[i]);
      if (ld) { vo2[i] = (u32)((l >> 3) * 1024 + (((l & 7) ^ ((g & 3) << 1)) * 16) + i * 7168); PIN(vo2[i]); }
    }
  };
  auto ubase = [&](int j) INL { return (u32)(j == 6 ? L_W3 : (j < 2 ? 2 * SLOT : j < 4 ? 0 : SLOT) + (j & 1) * 16384); };
  auto dma2 = [&](int j, int i) INL {
    SB0();
    if (i == 0) glds1<0>(rs2, j * 32768, vo2[0], ubase(j) + (u32)(v * 4096));
    if (i == 1) gldsn<1024>(rs2, j * 32768, vo2[1]);
    if (i == 2) gldsn<2048>(rs2, j * 32768, vo2[2]);
    if (i == 3) gldsn<3072>(rs2, j * 32768, vo2[3]);
    SB0();
  };
  v8s wf[7][4];
  auto w2rd = [&](int j, int dj) INL { wf[j][dj] = cat(trread(ra[dj] + ubase(j)), trread(ra[dj] + ubase(j) + 1024)); };
  auto w2read = [&](int j) INL {
#pragma unroll
    for (int dj = 0; dj < 4; ++dj) w2rd(j, dj);
  };
  u32 tag, eraw;
  asm volatile("s_load_dword %0, %1, 0x0" : "=s"(eraw) : "s"(ws) : "memory");
  auto gettag = [&]() INL { asm volatile("s_waitcnt lgkmcnt(0)\n\ts_add_u32 %0, %1, 1" : "=s"(tag) : "s"(eraw) : "scc", "memory"); };
  u32 vx[4], vw[4];
  auto init1 = [&]() INL {
    const u16 *xx = x, *ww = w1;
    asm volatile("" : "+s"(xx), "+s"(ww));
    rsX = mkrs((const char*)xx + mt1 * 128 * 1024, nrec); rsW = mkrs((const char*)ww + 256 * nt1, nrec);
#pragma unroll
    for (int i = 0; i < 4; ++i) {
      vx[i] = (u32)((32 * v + (l >> 3)) * 1024 + (((l & 7) ^ ((4 * (i & 1) + g) & 7)) * 16) + i * 7168);
      vw[i] = (u32)((16 * v + g) * 3584 + (((q4 ^ g) * 4 + p4) * 16) + i * (4 * 3584 - 1024));
    }
  };
  auto dma = [&](int c, int i) INL {
    const u32 base = (u32)((c % 3) * SLOT + v * 4096);
    const u32 kc = (u32)((c + (ROT1 == 1 ? (bi >> 2) + 2 * bi + (xcd >> 1) - (xcd & 1) : ROT1 == 2 ? (bi >> 2) + 2 * (xcd >> 1) - (xcd & 1) : ROT1 == 3 ? 2 * bi + (xcd >> 1) + 4 * (xcd & 1) : ROT1 == 4 ? xcd : ROT1 == 5 ? (bi >> 2) + 4 * (bi & 1) + (xcd >> 1) : 0)) & 7);
    SB0();
    if (i == 0) glds1<0>(rsX, kc * 128, vx[0], base);
    if (i == 1) gldsn<1024>(rsX, kc * 128, vx[1]);
    if (i == 2) gldsn<2048>(rsX, kc * 128, vx[2]);
    if (i == 3) gldsn<3072>(rsX, kc * 128, vx[3]);
    if (i == 4) glds1<0>(rsW, kc * (64 * 3584), vw[0], base + 16384);
    if (i == 5) gldsn<1024>(rsW, kc * (64 * 3584), vw[1]);
    if (i == 6) gldsn<2048>(rsW, kc * (64 * 3584), vw[2]);
    if (i == 7) gldsn<3072>(rsW, kc * (64 * 3584), vw[3]);
    SB0();
  };

  if (w < 4) {
    v4u xr[2];
    auto lnload = [&]() INL {
      if (LNNT) { ld4nt<0>(xr[0], l16, (const char*)x + (tb * 8 + 2 * v) * 1024); ld4nt<1024>(xr[1], l16, (const char*)x + (tb * 8 + 2 * v) * 1024); }
      else { ld4<0>(xr[0], l16, (const char*)x + (tb * 8 + 2 * v) * 1024); ld4<1024>(xr[1], l16, (const char*)x + (tb * 8 + 2 * v) * 1024); }
    };
    auto layernorm = [&](bool wd) INL {
      v2f e[2][4];
      float s[2], q[2], c0[2];
#pragma unroll
      for (int rr = 0; rr < 2; ++rr) {
        c0[rr] = bf2f((u32)__builtin_amdgcn_readfirstlane(xr[rr][0]));
#pragma unroll
        for (int j = 0; j < 4; ++j) e[rr][j] = (v2f){bf2f(xr[rr][j]), bfhi(xr[rr][j])} - c0[rr];
        const v2f s2 = (e[rr][0] + e[rr][1]) + (e[rr][2] + e[rr][3]);
        const v2f q2 = (e[rr][0] * e[rr][0] + e[rr][1] * e[rr][1]) + (e[rr][2] * e[rr][2] + e[rr][3] * e[rr][3]);
        s[rr] = s2[0] + s2[1]; q[rr] = q2[0] + q2[1];
        if (wd) { dma(1, 2 * rr); dma(1, 2 * rr + 1); }
      }
      auto ps = __builtin_amdgcn_permlane32_swap(f2u(s[0]), f2u(s[1]), false, false);
      auto pq = __builtin_amdgcn_permlane32_swap(f2u(q[0]), f2u(q[1]), false, false);
      auto pm = __builtin_amdgcn_permlane16_swap(f2u(u2f(ps[0]) + u2f(ps[1])), f2u(u2f(pq[0]) + u2f(pq[1])), false, false);
      float rv = u2f(pm[0]) + u2f(pm[1]);
      rv += dpp<0xB1>(rv); rv += dpp<0x4E>(rv); rv += dpp<0x141>(rv); rv += dpp<0x140>(rv);
      rv *= (1.0f / D);
      u32 sg[2];
      v4u o[2];
#pragma unroll
      for (int rr = 0; rr < 2; ++rr) {
        const float ms = u2f((u32)__builtin_amdgcn_readlane((int)f2u(rv), 32 * rr)), mq = u2f((u32)__builtin_amdgcn_readlane((int)f2u(rv), 32 * rr + 16));
        const float var = rbf(__builtin_fmaxf(mq - ms * ms, 0.f));
        const float mb = rbf(ms + c0[rr]);
        const float den = rbf(__builtin_amdgcn_sqrtf(rbf(var + 1e-5f)));
        const float rinv = __builtin_amdgcn_rcpf(den), off = (c0[rr] - mb) * rinv;
        if (wd) { dma(1, 4 + 2 * rr); dma(1, 5 + 2 * rr); }
#pragma unroll
        for (int j = 0; j < 4; ++j) o[rr][j] = pkh2(e[rr][j] * rinv + off);
        sg[rr] = (f2u(mb) >> 16) | f2u(den);
      }
      u32 lna = (u32)(L_LN + 2 * v * 1088) + l16;
      OPQ(lna);
      *LP(v4u, lna) = o[0]; *LP(v4u, lna + 1088) = o[1];
      gettag();
      st4c<0>((v4u){sg[0], tag, sg[1], tag}, 0u, ws + WS_STAT + (tb * 8 + 2 * v) * 8);
      STAMP(2);
      init2(false);
    };
    if (__builtin_expect(!hasg1, 0)) {
      lnload();
      asm volatile("s_barrier" ::: "memory");
      const int su = (xcd >> 1) * 4 + (bi & 3), cf0 = (xcd & 1) * 896 + (su < 14 ? su : 0) * 64;
      v4u cr[16];
      const char* cp = (const char*)w1 + (128 * v) * 3584 + cf0 * 2;
#pragma unroll
      for (int i = 0; i < 16; ++i) ld4<0>(cr[i], (u32)((l >> 3) * 3584 + (l & 7) * 16), cp + i * 8 * 3584);
      waitv<16>(); PIN(junk); PIN(xr[0]); PIN(xr[1]);
      layernorm(false);
      waitv<1>();
      v2f a[4];
#pragma unroll
      for (int j = 0; j < 4; ++j) a[j] = (v2f){0.f, 0.f};
#pragma unroll
      for (int i = 0; i < 16; ++i) {
        PIN(cr[i]);
#pragma unroll
        for (int j = 0; j < 4; ++j) a[j] += (v2f){bf2f(cr[i][j]), bfhi(cr[i][j])};
        if (i == 9) { SB0(); bar(); }
      }
      float r8[8];
#pragma unroll
      for (int j = 0; j < 8; ++j) { const float t = a[j >> 1][j & 1]; r8[j] = sum32x(sum16x(t + dpp<0x128>(t))); }
      if (l < 8) {
        *LP(v4f, L_CSP + v * 256 + l * 32) = (v4f){r8[0], r8[1], r8[2], r8[3]};
        *LP(v4f, L_CSP + v * 256 + l * 32 + 16) = (v4f){r8[4], r8[5], r8[6], r8[7]};
      }
      waitv<0>();
      bar();
      if (w == 0) {
        const float cs = (*LP(float, L_CSP + l * 4) + *LP(float, L_CSP + 256 + l * 4)) + (*LP(float, L_CSP + 512 + l * 4) + *LP(float, L_CSP + 768 + l * 4));
        st2c<0>((v2u){f2u(cs), tag}, (u32)(l * 8), ws + WS_CS + cf0 * 8);
      }
#pragma nounroll
      for (int c = 2; c <= 10; ++c) bar();
      w2read(0); w2read(1); w2read(2); w2read(3);
    } else {
      lnload();
      init1();
      asm volatile("s_barrier" ::: "memory");
#pragma unroll
      for (int i = 0; i < 8; ++i) dma(0, i);
      if (PF_MALL) touch(junk, (u32)((l >> 2) * 128), (const char*)w2 + ((b - 32) * 4 + v) * 2048);
      if (XPF) touch(junk, (u32)((l >> 2) * 128), (const char*)x + ((tb ^ 128) * 8 + 2 * v) * 1024);
      G1 s1;
      const int ti = v >> 1, fi = v & 1, h = l >> 5, r32 = l & 31;
#pragma unroll
      for (int i = 0; i < 4; ++i) { s1.xo[i] = (u32)((64 * ti + r32) * 128 + (((2 * i + h) ^ ((r32 >> 1) & 7)) * 16)); PIN(s1.xo[i]); }
#pragma unroll
      for (int i = 0; i < 2; ++i) { s1.wo[i] = (u32)(16384 + (8 * h + q4) * 256 + (((2 * fi + i) ^ q4) * 64) + (g & 1) * 32 + p4 * 8); PIN(s1.wo[i]); }
      u32 hoff = (u32)((g & 1) * 56 * 1024 + (h * 16 + c16) * 16);
      PIN(hoff);
      waitv<8 + PF_MALL + XPF>(); PIN(xr[0]); PIN(xr[1]);
      STAMP(1);
      layernorm(true);
#pragma unroll
      for (int i = 0; i < C2A; ++i) dma(2, i);
      waitv<9 + PF_MALL + C2A>();
      BETA(0);
      g1_step<false, false, true>(s1, s1.fb, s1.fa, 0u);
      waitv<1 + C2A>(); PIN(junk);
      BETA(1);
      g1_step<true, true, true>(s1, s1.fa, s1.fb, (u32)SLOT);
      waitv<0>();
      BETA(2);
      {
        u32 sb = 2 * SLOT;
#pragma nounroll
        for (int c = 2; c < 8; c += 2) {
          g1_step<false, true, true>(s1, s1.fb, s1.fa, sb);
          BETA(c + 1);
          sb = sb == 2 * SLOT ? 0u : sb + SLOT;
          g1_step<false, true, true>(s1, s1.fa, s1.fb, sb);
          BETA(c + 2);
          sb = sb == 2 * SLOT ? 0u : sb + SLOT;
        }
      }
      __builtin_amdgcn_s_setprio(2);
      u32 sta = (u32)(L_CSP + (64 * ti + r32) * 4), csa = (u32)(L_CSP + 512 + (64 * fi + r32) * 4);
      OPQ(sta); OPQ(csa);
      const u32 stg0 = *LP(u32, sta), stg1 = *LP(u32, sta + 128), csg0 = *LP(u32, csa), csg1 = *LP(u32, csa + 128);
      float den0, den1, ka0, ka1;
      v8s ca[2], mb[2];
      auto lastmm = [&](int m) INL {
        const int tbk = (m / 5) >> 1, fb = (m / 5) & 1, k = m % 5;
        s1.acc[tbk][fb] = k < 4 ? mfma32(s1.fb.w[k < 4 ? k : 0][fb], s1.fb.x[k < 4 ? k : 0][tbk], s1.acc[tbk][fb]) : mfma32(ca[fb], mb[tbk], s1.acc[tbk][fb]);
      };
      SB0();
      float cres[2], cb0[2];
#pragma unroll
      for (int m = 0; m < 16; ++m) {
        lastmm((m & 3) * 5 + (m >> 2));
        SB0();
        const int i = m & 1;
        if (m == 4 || m == 5) { const float cs = u2f(i ? csg1 : csg0); cb0[i] = rbf(cs); cres[i] = cs - cb0[i]; PIN(cres[i]); PIN(cb0[i]); }
        if (m == 6 || m == 7) {
          const float c1 = rbf(cres[i]);
          float c2v = cres[i] - c1; OPQ(c2v);
          u32 z0 = 0, z1 = 0; PIN(z0); PIN(z1);
          ca[i] = __builtin_bit_cast(v8s, (v4u){(f2u(cb0[i]) >> 16) | f2u(c1), pkbf(c2v, 0.f), z0, z1}); PIN(ca[i]);
        }
        if (m == 8 || m == 9) {
          const u32 nm = h ? 0u : ((i ? stg1 : stg0) & 0xffffu) ^ 0x8000u;
          u32 z0 = 0, z1 = 0; PIN(z0); PIN(z1);
          mb[i] = __builtin_bit_cast(v8s, (v4u){nm | (nm << 16), nm, z0, z1}); PIN(mb[i]);
        }
        if (m == 10) { den0 = bfhi(stg0); ka0 = -1.44269504089f * __builtin_amdgcn_rcpf(den0); PIN(ka0); PIN(den0); }
        if (m == 11) { den1 = bfhi(stg1); ka1 = -1.44269504089f * __builtin_amdgcn_rcpf(den1); PIN(ka1); PIN(den1); }
        SB0();
      }
      lastmm(4); lastmm(9); lastmm(14); lastmm(19);
      SB0();
      char* hbase = ws + WS_H + ((mt1 * 8 + 4 * ti) * 56 + 4 * nt1 + 2 * fi) * 1024;
      auto epi = [&](int tbk, int fb, float den, float ka) INL {
        const v16f a = s1.acc[tbk][fb];
        u32 hp[8];
#pragma unroll
        for (int j = 0; j < 8; ++j) {
          const v2f t = {a[2 * j], a[2 * j + 1]};
          const v2f ar = t * ka;
          const v2f d = (v2f){__builtin_amdgcn_exp2f(ar[0]), __builtin_amdgcn_exp2f(ar[1])} * den + den;
          hp[j] = pkbf2(t * (v2f){__builtin_amdgcn_rcpf(d[0]), __builtin_amdgcn_rcpf(d[1])});
          if (j == 3) st4c<0>((v4u){hp[0], hp[1], hp[2], hp[3]}, hoff, hbase + (tbk * 112 + fb) * 1024);
        }
        st4c<512>((v4u){hp[4], hp[5], hp[6], hp[7]}, hoff, hbase + (tbk * 112 + fb) * 1024);
      };
      STAMP(21);
      asm volatile("s_barrier" ::: "memory");
      epi(0, 0, den0, ka0);
      ESTAMP(3);
      if (GAM == 1) asm volatile("s_barrier" ::: "memory");
      epi(0, 1, den0, ka0);
      ESTAMP(4);
      if (GAM == 2) asm volatile("s_barrier" ::: "memory");
      epi(1, 0, den1, ka1);
      ESTAMP(5);
      epi(1, 1, den1, ka1);
      ESTAMP(6);
      w2read(0); w2read(1);
      waitv<4>();
      st1c<0>(tag, (u32)(((mt1 * 4 + 2 * ti) * 32 + 2 * nt1 + fi) * 128 + FULLF * r32 * 4), ws + WS_FH);
      w2read(2); w2read(3);
      waitv<1>();
      st1c<0>(tag, (u32)(((mt1 * 4 + 2 * ti + 1) * 32 + 2 * nt1 + fi) * 128 + FULLF * r32 * 4), ws + WS_FH);
      __builtin_amdgcn_s_setprio(0);
    }
    STAMP(22);
    bar();
    STAMP(23);
  } else {
    u32 pA, pI, pR, pC, pR2, pC2, pdt;
    const int kch = v >> 1;
    {
      const u32 po = (u32)((tb * 2 * NS + v * 64 + l) * 2);
      ldus(pA, po, logA); ldus(pI, po, Aim); ldus(pR, po, Cre); ldus(pC, po, Cim);
      ldus(pR2, po ^ 128u, Cre); ldus(pC2, po ^ 128u, Cim); ldus(pdt, (u32)((tb * 2 + kch) * 2), logdt);
    }
    asm volatile("s_barrier" ::: "memory");
    init1();
#pragma unroll
    for (int i = 0; i < 4; ++i) { PIN(vx[i]); PIN(vw[i]); }
    init2(true);
    u32 ta = (u32)(L_TA + (v * 64 + l) * 128 + (((l & 1) | ((l >> 1) & 2) | ((l << 1) & 4)) * 16));
    u32 tbb = (u32)(L_TB + (v * 64 + l) * TB_P);
    PIN(ta); PIN(tbb);
    waitv<0>();
    PIN(pA); PIN(pI); PIN(pR); PIN(pC); PIN(pR2); PIN(pC2); PIN(pdt);
    STAMP(1);
    const float dt = rbf(__builtin_amdgcn_exp2f(bf2f(pdt) * 1.44269504089f));
    const float ar = -rbf(__builtin_amdgcn_exp2f(bf2f(pA) * 1.44269504089f)) * dt * 1.44269504089f;
    const float ai = bf2f(pI) * dt * 0.15915494309f;
    u32 ex = wmaxu(umax(umax(pR & 0x7f80u, pC & 0x7f80u), umax(pR2 & 0x7f80u, pC2 & 0x7f80u))) >> 7;
    ex = ex < 8u ? 8u : (ex > 250u ? 250u : ex);
    const float sc = u2f((260u - ex) << 23);
    const float unsc = u2f((ex - 6u) << 23) * 2.0f * dt;
    const cf C = {bf2f(pR) * sc, bf2f(pC) * sc};
    const cf z = cexp(ar, ai, 1.f), z8 = cexp(ar, -ai, 8.f);
    const cf W = cexp(ar, ai, 128.f);
    cf U[8], Y[8], Z[8], W2, s6, s7, last, dum;
    v4u lt[7];
    auto pack = [&](const cf* p, int k) INL {
#pragma unroll
      for (int j = 0; j < 4; ++j) { lt[k][j] = pkh(p[2 * j].r, p[2 * j + 1].r); lt[k + 3][j] = pkh(p[2 * j].i, p[2 * j + 1].i); }
      PIN(lt[k]); PIN(lt[k + 3]);
    };
    auto late = [&](int k) INL {
      if (k == 0) { PIN(W2.r); cmul2(U[0], s6, U[1], s7, W2); }
      if (k == 1) cmul2(U[2], U[0], U[3], U[1], W2);
      if (k == 2) cmul2(U[4], U[2], U[5], U[3], W2);
      if (k == 3) cmul2(U[6], U[4], U[7], U[5], W2);
      if (k == 4) pack(U, 0);
      if (k == 5) { cmul2(last, U[6], dum, U[7], W2); lt[6] = (v4u){pkh(last.r, 0.f), 0u, pkh(last.i, 0.f), 0u}; PIN(lt[6]); }
      if (k == 6) { PIN(Y[2].r); cmul2(Y[3], Y[1], Y[4], Y[2], Y[2]); }
      if (k == 7) cmul2(Y[5], Y[3], Y[6], Y[4], Y[2]);
      if (k == 8) cmul2(Y[7], Y[5], Z[0], Y[6], Y[2]);
      if (k == 9) pack(Y, 1);
      if (k == 10) cmul2(Z[1], Y[1], Z[2], Y[2], Z[0]);
      if (k == 11) cmul2(Z[3], Y[3], Z[4], Y[4], Z[0]);
      if (k == 12) cmul2(Z[5], Y[5], Z[6], Y[6], Z[0]);
      if (k == 13) cmul2(Z[7], Y[7], dum, Y[7], Z[0]);
      if (k == 14) pack(Z, 2);
      SB0();
    };
    {
      auto putA = [&](int chunk, const c2* p) INL {
        *LP(v4u, ta ^ (u32)(chunk * 16)) = (v4u){pkh2(p[0].r), pkh2(p[1].r), pkh2(p[2].r), pkh2(p[3].r)};
        *LP(v4u, ta ^ (u32)(chunk * 16 + 64)) = (v4u){pkh2(p[0].i), pkh2(p[1].i), pkh2(p[2].i), pkh2(p[3].i)};
      };
      c2 P[4], Q[4], R[4], S[4];
      const cf z2 = cmul(z, z), cz = cmul(C, z);
      W2 = cmul(W, W); Y[0] = {1.f, 0.f}; Y[1] = z8; Y[2] = cmul(z8, z8);
      P[0] = {(v2f){C.r, cz.r}, (v2f){C.i, cz.i}};
#pragma unroll
      for (int j = 1; j < 4; ++j) P[j] = mulS(P[j - 1], z2);
#pragma unroll
      for (int j = 0; j < 4; ++j) Q[j] = mulS(P[j], W);
      putA(0, P);
      R[0] = mulS({(v2f){Q[0].r[0], Q[1].r[0]}, (v2f){Q[0].i[0], Q[1].i[0]}}, W);
      R[1] = mulS({(v2f){Q[2].r[0], Q[3].r[0]}, (v2f){Q[2].i[0], Q[3].i[0]}}, W);
      R[2] = mulS(R[0], W); R[3] = mulS(R[1], W);
      putA(1, Q);
      S[0] = mulS({(v2f){R[2].r[0], R[3].r[0]}, (v2f){R[2].i[0], R[3].i[0]}}, W);
#pragma unroll
      for (int j = 1; j < 4; ++j) S[j] = mulS(S[j - 1], W);
      putA(2, R);
      putA(3, S);
      s6 = {S[2].r[0], S[2].i[0]}; s7 = {S[3].r[0], S[3].i[0]};
      PIN(s6.r); PIN(s6.i); PIN(s7.r); PIN(s7.i); PIN(W2.r); PIN(W2.i); PIN(Y[1].r); PIN(Y[1].i); PIN(Y[2].r); PIN(Y[2].i);
    }
    __builtin_amdgcn_s_setprio(1);
    STAMP(2);
    BETA(0);
    gettag();
    {
      u32 lnt = (u32)(L_LN + q4 * 1088 + v * 256 + g * 32 + p4 * 8);
      OPQ(lnt);
      const v8s t0 = cat(trread(lnt), trread(lnt + 4 * 1088)), t1 = cat(trread(lnt + 128), trread(lnt + 128 + 4 * 1088));
#pragma unroll
      for (int i = C2A; i < 8; ++i) dma(2, i);
      st4c<0>(__builtin_bit_cast(v4u, t0), l16, ws + WS_XNB + tb * 8192 + v * 2048);
      st4c<1024>(__builtin_bit_cast(v4u, t1), l16, ws + WS_XNB + tb * 8192 + v * 2048);
    }
    const char* pfb = (const char*)w2 + (896 * kh + 224 * (bi >> 3) + (bi & 7) * 4 + v) * 1024;
    auto pf = [&](int it) INL { if (PF_W2) touch(junk, (u32)((l >> 3) * 128), pfb + it * 32768); };
    pf(0);
    BETA(1);
    u32 ka[11], a0[2], a2, ay, kw0, kw1, kc, ro, ua;
    const int kt = v & 1, ri = q4 & 1;
    const u32 kb = (u32)(L_KD + kch * KD_P + 8);
    const int lagtop = 2047 - 8 * (tid & 255);
    const float lf = (float)lagtop;
    const bool big = lagtop >= 160;
    auto kaddr = [&](int e) INL {
      float f0 = lf - (float)e, f1 = lf - (float)(e + 1);
      OPQ(f0);
      const u32 pk = pkbf(f0, f1);
      ka[e] = (u32)(L_KD + 8 + 2 * (big ? (int)(pk & 0xffffu) - 17024 : lagtop - e));
      PIN(ka[e]);
      if (e + 1 < 11) { ka[e + 1] = (u32)(L_KD + 8 + 2 * (big ? (int)(pk >> 16) - 17024 : lagtop - e - 1)); PIN(ka[e + 1]); }
    };
#pragma unroll
    for (int c = 3; c < 8; ++c) {
#pragma unroll
      for (int i = 0; i < 8; ++i) {
        dma(c, i);
        if (c < 6 && (i == 2 || i == 5)) kaddr((c - 3) * 4 + (i == 5 ? 2 : 0));
        if (c == 6 && i == 2) {
#pragma unroll
          for (int t = 0; t < 2; ++t) {
            const int n = 4 * g + 2 * t + (q4 >> 1);
            a0[t] = (u32)(L_TA + (kch * 128 + n) * 128 + (((ri * 4 + 2 * kt + (p4 >> 1)) ^ ((n & 1) | ((n >> 1) & 2) | ((n << 1) & 4))) * 16) + (p4 & 1) * 8);
            PIN(a0[t]);
          }
        }
        if (c == 6 && i == 5) {
          const u32 rb = (u32)(L_TB + (kch * 128 + 4 * g + (q4 >> 1)) * TB_P);
          a2 = rb + (u32)(p4 < 2 ? ri * 48 + p4 * 8 : 96 + ri * 8);
          ay = rb + (u32)(ri * 48 + 16 + p4 * 8);
          PIN(a2); PIN(ay);
        }
        if (c == 7 && i == 2) {
          const int base = kt == 0 ? 128 * (g >> 1) + 4 * (g & 1) + 8 * c16 : (g < 2 ? 256 + 64 * g + 4 * c16 : 256 + 64 * g + 2 * c16);
          kw0 = kb + base * 2; kw1 = kw0 + ((kt == 1 && g >= 2) ? 64 : 4);
          PIN(kw0); PIN(kw1);
        }
        if (c == 7 && i == 5) {
          kc = kb + (u32)((512 + 64 * g + c16) * 2);
          ro = (u32)(L_R + (tid & 255) * 16); ua = (u32)(L_U + (tid & 1) * 4608 + 512 + ((tid & 255) >> 1) * 16);
          PIN(kc); PIN(ro); PIN(ua);
        }
      }
      late(3 * (c - 3)); late(3 * (c - 3) + 1); late(3 * (c - 3) + 2);
      pf(c - 2);
      if (c == 3 && FLA == 3) waitv<10>(); else waitv<8>();
      BETA(c - 1);
      if (c == FLA + 1 && w == 4) st1c<0>(tag, 0u, ws + WS_FA + tb * 4);
    }
    v4u pollA, uv0, uv1; v2u gr;
    const char* grp = ws + (v < 2 ? WS_STAT + (mt1 * 128 + 64 * v) * 8 : WS_CS + (nt1 * 128 + 64 * (v - 2)) * 8);
    ld4c<0>(pollA, l16, ws + WS_FA);
    ld2c<0>(gr, (u32)(l * 8), grp);
    pf(6);
#pragma unroll
    for (int e = 0; e < 8; ++e) dma2(e >> 2, e & 3);
    waitv<10 + 2 * PF_W2>(); BETA(7);
    v8s yb[8], xk[8], xj[8];
#pragma unroll
    for (int e = 0; e < 8; ++e) {
      dma2(2 + (e >> 2), e & 3);
      if (e >= 4) {
        xk[2 * e - 8] = cat(trread(a0[0] + (2 * e - 8) * 2048), trread(a0[1] + (2 * e - 8) * 2048));
        xk[2 * e - 7] = cat(trread(a0[0] + (2 * e - 7) * 2048), trread(a0[1] + (2 * e - 7) * 2048));
      }
    }
    waitv<16>(); PIN(pollA); PIN(gr); PIN(junk);
    if (__builtin_expect(!wall(gr[1] == tag), 0)) {
      for (u32 spins = 0; spins < kSpin; ++spins) {
        __builtin_amdgcn_s_sleep(4);
        ld2c<0>(gr, (u32)(l * 8), grp);
        waitv<0>(); PIN(gr);
        if (wall(gr[1] == tag)) break;
      }
    }
    *LP(u32, L_CSP + (tid & 255) * 4) = gr[0];
    if (PDEC) BETA(8);
    if (__builtin_expect(!wall(pollA[0] == tag && pollA[1] == tag && pollA[2] == tag && pollA[3] == tag), 0)) {
      for (u32 spins = 0; spins < kSpin; ++spins) {
        __builtin_amdgcn_s_sleep(PAS);
        ld4c<0>(pollA, l16, ws + WS_FA);
        waitv<0>(); PIN(pollA);
        if (wall(pollA[0] == tag && pollA[1] == tag && pollA[2] == tag && pollA[3] == tag)) break;
      }
    }
    if (!PDEC) BETA(8);
    __builtin_amdgcn_s_setprio(3);
    if (b == 0 && w == 4) st1c<0>(tag, 0u, ws + WS_E);
    ld4c<0>(uv0, (u32)(((tid & 255) >> 1) * 8192 + (tid & 1) * 16), ws + WS_XNB + tb * 32);
    ld4c<0>(uv1, (u32)(((tid & 255) >> 1) * 8192 + (tid & 1) * 16), ws + WS_XNB + tb * 32 + 128 * 8192);
#pragma unroll
    for (int j = 0; j < 7; ++j) *LP(v4u, tbb + j * 16) = lt[j];
    bar();
    {
      v4f k0 = {0.f, 0.f, 0.f, 0.f}, k2 = k0;
#pragma unroll
      for (int s = 0; s < 8; ++s) {
        yb[s] = cat(trread(ay + s * 16 * TB_P), trread(ay + s * 16 * TB_P + 2 * TB_P));
        xj[s] = cat(trread(a2 + s * 16 * TB_P), trread(a2 + s * 16 * TB_P + 2 * TB_P));
        dma2(4 + (s >> 2), s & 3);
        if (!KLATE && s >= 2) { k0 = mfma_h(xk[s - 2], yb[s - 2], k0); k2 = mfma_h(xj[s - 2], yb[s - 2], k2); }
      }
      if (KLATE == 2) __builtin_amdgcn_s_setprio(1);
#pragma unroll
      for (int s = KLATE ? 0 : 6; s < 8; ++s) { k0 = mfma_h(xk[s], yb[s], k0); k2 = mfma_h(xj[s], yb[s], k2); }
      SB0();
      if (KLATE == 2) __builtin_amdgcn_s_setprio(3);
      waitv<10>();
      *LP(u32, kw0) = pkh(k0[0], k0[1]); *LP(u32, kw1) = pkh(k0[2], k0[3]);
      const u32 k01 = pkh(k2[0], k2[1]), k23 = pkh(k2[2], k2[3]);
      if (g < 3) {
        *LP(u16, kc) = (u16)k01; *LP(u16, kc + 32) = (u16)(k01 >> 16);
        *LP(u16, kc + 64) = (u16)k23; *LP(u16, kc + 96) = (u16)(k23 >> 16);
      }
      if (l == 0) { *LP(v2u, kb - 8) = (v2u){0u, 0u}; *LP(float, L_US + kch * 4) = unsc; }
    }
    __builtin_amdgcn_s_setprio(1);
    STAMP(21);
    bar();
    u32 kv[2][11];
#pragma unroll
    for (int e = 0; e < 11; ++e) {
      if (e < 4) dma2(6, e);
      kv[0][e] = *LP(u16, ka[e]); kv[1][e] = *LP(u16, ka[e] + KD_P);
    }
    if (BPRI) __builtin_amdgcn_s_setprio(3);
    auto expand = [&](int ch) INL {
      u32 pr[10];
#pragma unroll
      for (int e = 0; e < 10; ++e) pr[e] = kv[ch][e] | (kv[ch][e + 1] << 16);
#pragma unroll
      for (int sg = 0; sg < 4; ++sg)
        *LP(v4u, ro + (ch * 4 + sg) * 4160) = (v4u){pr[3 - sg], pr[5 - sg], pr[7 - sg], pr[9 - sg]};
    };
    expand(0);
    expand(1);
    waitv<12>(); PIN(uv0); PIN(uv1);
    *LP(v4u, ua) = uv0; *LP(v4u, ua + 2048) = uv1;
    {
      const int i = tid & 255;
      if (i < 16) *LP(v4u, L_R + (i >> 1) * 4160 + 4096 + (i & 1) * 16) = (v4u){0u, 0u, 0u, 0u};
      if (i >= 64 && i < 128) *LP(v4u, L_U + ((i >> 5) & 1) * 4608 + (i & 31) * 16) = (v4u){0u, 0u, 0u, 0u};
    }
    w2read(0); w2read(1); w2read(2); w2read(3);
    waitv<0>();
    __builtin_amdgcn_s_setprio(0);
    STAMP(22);
    bar();
    STAMP(23);
  }
  asm volatile("" : "+s"(w), "+s"(kh), "+s"(mt2), "+s"(dt2), "+s"(tb));
  __builtin_amdgcn_s_setprio(3);
  const int tt2 = 2 * mt2 + kh;
  v4u hf[4][7];
  const char* hsrc = ws + WS_H + ((mt2 * 8 + (w >> 2) * 2) * 56 + 28 * kh + 7 * (w & 3)) * 1024 + 3072;
  auto hld = [&](int ti, int j) INL {
    const char* p = hsrc + ((ti & 1) + 4 * (kh ^ (ti < 2 ? 1 : 0))) * 56 * 1024;
    if (j == 0) ld4c<-3072>(hf[ti][0], l16, p);
    if (j == 1) ld4c<-2048>(hf[ti][1], l16, p);
    if (j == 2) ld4c<-1024>(hf[ti][2], l16, p);
    if (j == 3) ld4c<0>(hf[ti][3], l16, p);
    if (j == 4) ld4c<1024>(hf[ti][4], l16, p);
    if (j == 5) ld4c<2048>(hf[ti][5], l16, p);
    if (j == 6) ld4c<3072>(hf[ti][6], l16, p);
  };
  auto hslot = [&](int slot) INL {
#pragma unroll
    for (int k = 0; k < 28; ++k) if (SCH.h[k] == slot) hld(k / 7, k % 7);
  };
  const char* fsrc = ws + WS_FH + ((mt2 * 4 + (w >> 2)) * 32 + 14 * kh + ((7 * (w & 3)) >> 1)) * 128;
  const u32 foff = (u32)((l & 3) * 128 + (l & 4) * 2048);
  const u32 ooff = (u32)((((w >> 2) * 32 + (w & 3) * 4 + g) * D + c16 * 4) * 2);
  u32 pollH, dsp; v2u xv0, xv1;
  {
    const int ch = w >> 2;
    v4f ya[8];
    const v4f zero4 = {0.f, 0.f, 0.f, 0.f};
    u32 fb = (u32)(L_R + (ch * 4 + (c16 & 3)) * 4160 + (2028 - 32 * 57 - 64 * v + 8 * g - 4 * q4) * 2);
    u32 gb = (u32)(L_U + ch * 4608 + 512 + (16 * c16 - 16 + 8 * g - 64 * v - 32) * 2);
    OPQ(fb); OPQ(gb);
    auto ldF = [&](int qq, int i) INL { const u32 a = fb + (1 - qq) * 64 + (7 - i) * 512; return cat(*(volatile LAS v4s*)LP(v4s, a), *(volatile LAS v4s*)LP(v4s, a + 8)); };
    auto ldG = [&](int qq, int i) INL { return *LP(v8s, gb + (1 - qq) * 64 + i * 512); };
    u32 pw = (u32)((w < 4 ? 2 * SLOT : 0) + (w & 3) * 8192 + (g * 16 + ((c16 + 4 * g) & 15)) * 16);
    OPQ(pw);
    auto pwr = [&](int n) INL { *LP(v4f, pw + n * 1024) = ya[n]; };
    v8s Gf[8], Ff[8], F2[8], G2[8];
    Ff[0] = ldF(0, 0);
#pragma unroll
    for (int i = 0; i < 8; ++i) Gf[i] = ldG(0, i);
    Ff[1] = ldF(0, 1);
#pragma unroll
    for (int i = 0; i < 8; ++i) {
      SB0();
#pragma unroll
      for (int n = i; n < 8; ++n) {
        ya[n] = mfma_h(Ff[i], Gf[n - i], i == 0 ? zero4 : ya[n]);
        const int m = i * 8 - i * (i - 1) / 2 + (n - i);
        SB0();
        if (m >= 4 && m < 16 && (m & 1) == 0) Ff[2 + (m - 4) / 2] = ldF(0, 2 + (m - 4) / 2);
        if (m >= 16 && m < 36 && (m & 3) == 0) { F2[(m - 16) / 4] = ldF(1, (m - 16) / 4); G2[(m - 16) / 4] = ldG(1, (m - 16) / 4); }
        SB0();
      }
      if (i == 0) {
        ld2<0>(xv0, ooff, (const char*)x + (tt2 * 64 * D + dt2 * 64) * 2);
        ld2<0>(xv1, ooff, (const char*)x + ((tt2 * 64 + 16) * D + dt2 * 64) * 2);
        ld1c<0>(dsp, 0u, (const char*)dskip + tb * 4);
      }
      if (CPRIO && i == 1) __builtin_amdgcn_s_setprio(2);
      if (POLLM < 0 && i == -(POLLM)) ld1c<0>(pollH, foff, fsrc);
    }
    STAMP(24);
    if (CPRIO) __builtin_amdgcn_s_setprio(1);
    auto tile = [&](int n) INL {
      if (n >= 2 && n + 3 < 8) { F2[n + 3] = ldF(1, n + 3); G2[n + 3] = ldG(1, n + 3); }
      SB0();
#pragma unroll
      for (int i = 0; i <= n; ++i) {
        ya[n] = mfma_h(F2[i], G2[n - i], ya[n]);
        const int m = n * (n + 1) / 2 + i - 10;
        SB0();
        if (i == 1 && n >= 2) pwr(n - 1);
        if (i == 0 && n == 2) pwr(0);
        if (m >= 12 && m < 24) w2rd(4 + ((m - 12) >> 2), (m - 12) & 3);
        hslot(m);
        if (m + 10 == POLLM) ld1c<0>(pollH, foff, fsrc);
        SB0();
      }
    };
#pragma unroll
    for (int n = 0; n < CKT; ++n) tile(n);
    if (CKT == 8) pwr(7);
    waitv<0>(); PIN(pollH); PIN(xv0); PIN(xv1); PIN(dsp);
    if (__builtin_expect(!wall(pollH == tag), 0)) {
      for (u32 spins = 0; spins < kSpin; ++spins) {
        ld1c<0>(pollH, foff, fsrc);
        waitv<0>(); PIN(pollH);
        if (wall(pollH == tag)) break;
        __builtin_amdgcn_s_sleep(2);
      }
    }
    STAMP(25);
#pragma unroll
    for (int n = CKT; n < 8; ++n) tile(n);
    if (CKT < 8) pwr(7);
    hslot(29);
  }
  __builtin_amdgcn_s_setprio(3);
  bar();
  STAMP(26);
  {
    u32 pa = (u32)(w * 1024 + ((l & 3) * 16 + (((l >> 2) + 4 * (l & 3)) & 15)) * 16), pb = pa + 2 * SLOT;
    OPQ(pa); OPQ(pb);
    v4f rp[8];
#pragma unroll
    for (int k = 0; k < 4; ++k) { rp[k] = *LP(v4f, pb + k * 8192); rp[4 + k] = *LP(v4f, pa + k * 8192); }
    u32 ul = (u32)(L_U + 512 + tid * 8);
    OPQ(ul);
    const v4h u0 = *LP(v4h, ul), u1 = *LP(v4h, ul + 4608);
    const v2f us = *LP(v2f, L_US);
    hslot(30);
    u32 o[4];
    {
      const float ds0 = bf2f(dsp), ds1 = bfhi(dsp);
#pragma unroll
      for (int j = 0; j < 4; ++j) {
        o[j] = pkbf(fma1(sum4(rp[0][j], rp[1][j], rp[2][j], rp[3][j]), us[0], mul1((float)u0[j], ds0)),
                    fma1(sum4(rp[4][j], rp[5][j], rp[6][j], rp[7][j]), us[1], mul1((float)u1[j], ds1)));
        PIN(o[j]);
      }
    }
    auto sstore = [&]() INL {
      char* sdst = ws + WS_SG + (w * 4 * 256 + tb) * 512;
      const u32 so = (u32)(g * 256 * 512 + c16 * 16);
      st4c<0>((v4u){o[0], tag, o[1], tag}, so, sdst);
      st4c<256>((v4u){o[2], tag, o[3], tag}, so, sdst);
    };
    hslot(31);
    if (STPOS == 31) sstore();
    STAMP(27);
    const u32 qw = (u32)(w * 4096 + c16 * 256);
    v4f a2[2][4];
    auto qbase = [&](int ti) INL { return (u32)(ti == 0 ? L_R : ti == 1 ? SLOT : ti == 2 ? 0 : 2 * SLOT); };
    auto qwr = [&](int ti, int dj) INL { *LP(v4f, qbase(ti) + qw + (((dj * 4 + g) ^ (c16 & 7)) * 16)) = a2[ti & 1][dj]; };
    const int rt = (w & 3) * 4 + g;
    const u32 rdo = (u32)((w >> 2) * 16384 + rt * 256 + ((c16 ^ (rt & 7)) * 16));
    auto redld = [&](int t0) INL {
      u32 a0 = qbase(t0) + rdo, a1 = qbase(t0 + 1) + rdo;
      OPQ(a0); OPQ(a1);
#pragma unroll
      for (int k = 0; k < 4; ++k) rp[k] = *LP(v4f, a0 + k * 4096);
#pragma unroll
      for (int k = 0; k < 4; ++k) rp[4 + k] = *LP(v4f, a1 + k * 4096);
    };
    const int xo = (w >> 2) * 8192 + (w & 3) * 1024;
    auto xsend = [&](int r) INL {
      const u32 d0 = pkbf(sum4(rp[4 * r][0], rp[4 * r + 1][0], rp[4 * r + 2][0], rp[4 * r + 3][0]), sum4(rp[4 * r][1], rp[4 * r + 1][1], rp[4 * r + 2][1], rp[4 * r + 3][1]));
      const u32 d1 = pkbf(sum4(rp[4 * r][2], rp[4 * r + 1][2], rp[4 * r + 2][2], rp[4 * r + 3][2]), sum4(rp[4 * r][3], rp[4 * r + 1][3], rp[4 * r + 2][3], rp[4 * r + 3][3]));
      st4c<0>((v4u){d0, tag, d1, tag}, l16, ws + WS_XG + ((tt2 ^ 1) * 8 + dt2) * 16384 + xo + r * 4096);
    };
    v4u sg0, sg1, xg0, xg1;
    const char* ssrc = ws + WS_SG + (tt2 * 256 + dt2 * 32) * 512;
    const char* xsrc = ws + WS_XG + (tt2 * 8 + dt2) * 16384 + xo;
    auto g2 = [&](int ti) INL {
      const v4f zero4 = {0.f, 0.f, 0.f, 0.f};
#pragma unroll
      for (int j = 0; j < 7; ++j) {
        waitvn(SCH.wait[ti * 7 + j]);
        PIN(hf[ti][j]);
#pragma unroll
        for (int dj = 0; dj < 4; ++dj) {
          a2[ti & 1][dj] = mfma_bf(wf[j][dj], __builtin_bit_cast(v8s, hf[ti][j]), j == 0 ? zero4 : a2[ti & 1][dj]);
          SB0();
          const int slot = 40 + ti * 28 + j * 4 + dj;
          hslot(slot);
          if (STPOS == slot) sstore();
          if (XS0 == slot) xsend(0);
          if (XS1 == slot) xsend(1);
          if (SGPOS == slot) { ld4c<0>(sg0, (u32)(tid * 16), ssrc); ld4c<0>(sg1, (u32)(tid * 16), ssrc + 8192); }
          if (XGPOS == slot) { ld4c<0>(xg0, l16, xsrc); ld4c<0>(xg1, l16, xsrc + 4096); }
          if (dj == 1 && (ti & 1) && j >= 1 && j < 5) qwr(ti - 1, j - 1);
          if (ti == 2 && j < 2 && (dj & 1)) qwr(1, j * 2 + (dj >> 1));
          if (ti == 2 && j == 2 && dj == 1) { bar(); redld(0); }
          SB0();
        }
      }
    };
    __builtin_amdgcn_s_setprio(2);
    g2(0);
    ESTAMP(7);
    __builtin_amdgcn_s_setprio(1);
    g2(1);
    ESTAMP(8);
    g2(2);
    STAMP(28);
    u32 sr = (u32)(L_S + ((w >> 2) * 16 + (rt >> 1)) * 136 + (rt & 1) * 4480 + c16 * 8);
    u32 sa = (u32)(L_S + (2 * c16 + (g & 1)) * 136 + (tid >> 5) * 4);
    PIN(sr); PIN(sa);
    waitv<sgwait()>(); PIN(sg0); PIN(sg1);
    if (__builtin_expect(!wall(sg0[1] == tag && sg0[3] == tag && sg1[1] == tag && sg1[3] == tag), 0)) {
      for (u32 spins = 0; spins < kSpin; ++spins) {
        __builtin_amdgcn_s_sleep(SGS);
        ld4c<0>(sg0, (u32)(tid * 16), ssrc); ld4c<0>(sg1, (u32)(tid * 16), ssrc + 8192);
        waitv<0>(); PIN(sg0); PIN(sg1);
        if (wall(sg0[1] == tag && sg0[3] == tag && sg1[1] == tag && sg1[3] == tag)) break;
      }
    }
    *LP(u32, sa) = sg0[0]; *LP(u32, sa + 4480) = sg0[2];
    *LP(u32, sa + 64) = sg1[0]; *LP(u32, sa + 64 + 4480) = sg1[2];
    ESTAMP(9);
    __builtin_amdgcn_s_setprio(0);
    g2(3);
    ESTAMP(10);
#pragma unroll
    for (int dj = 0; dj < 4; ++dj) qwr(3, dj);
    ESTAMP(12);
    bar();
    STAMP(29);
    redld(2);
    const v2u sa0 = *LP(v2u, sr), sa1 = *LP(v2u, sr + 8 * 136);
    waitv<0>(); PIN(xg0); PIN(xg1);
    ESTAMP(11);
    if (__builtin_expect(!wall(xg0[1] == tag && xg0[3] == tag && xg1[1] == tag && xg1[3] == tag), 0)) {
      for (u32 spins = 0; spins < kSpin; ++spins) {
        if (XGS) __builtin_amdgcn_s_sleep(XGS);
        ld4c<0>(xg0, l16, xsrc); ld4c<0>(xg1, l16, xsrc + 4096);
        waitv<0>(); PIN(xg0); PIN(xg1);
        if (wall(xg0[1] == tag && xg0[3] == tag && xg1[1] == tag && xg1[3] == tag)) break;
      }
    }
    auto fin = [&](v4f f, v4u pq, v2u ss, v2u xx) INL {
      f += (v4f){bf2f(pq[0]), bfhi(pq[0]), bf2f(pq[2]), bfhi(pq[2])};
      v2u o2;
#pragma unroll
      for (int j = 0; j < 2; ++j)
        o2[j] = pkbf(bf2f(ss[j]) * silu(f[2 * j]) + bf2f(xx[j]), bfhi(ss[j]) * silu(f[2 * j + 1]) + bfhi(xx[j]));
      return o2;
    };
    char* ob = (char*)out + (tt2 * 64 * D + dt2 * 64) * 2;
    st2o<0>(fin((rp[0] + rp[1]) + (rp[2] + rp[3]), xg0, sa0, xv0), ooff, ob);
    st2o<0>(fin((rp[4] + rp[5]) + (rp[6] + rp[7]), xg1, sa1, xv1), ooff, ob + 16 * D * 2);
  }
  STAMP(30);
}

extern "C" void kernel_launch(void* const* d_in, const int* in_sizes, int n_in, void* d_out, int out_size,
                              void* d_ws, size_t ws_size, hipStream_t stream) {
  fused_kernel<<<256, 512, 0, stream>>>((const u16*)d_in[0], (const u16*)d_in[1], (const u16*)d_in[2],
                                        (const u16*)d_in[3], (const u16*)d_in[4], (const u16*)d_in[5],
                                        (const u16*)d_in[6], (const u16*)d_in[8], (const u16*)d_in[9],
                                        (u16*)d_out, (char*)d_ws);
}
```
